```python
import torch

def _softmax(x, axis=-1):
    return torch.softmax(x, dim=axis)
def _gelu(x):
    return torch.nn.functional.gelu(x, approximate="tanh")
def _silu(x):
    return torch.nn.functional.silu(x)
def _sigmoid(x):
    return torch.sigmoid(x)
def _layernorm(x, axis=-1, eps=1e-5):
    m = x.mean(dim=axis, keepdim=True)
    v = x.var(dim=axis, keepdim=True, unbiased=False)
    return (x - m) / torch.sqrt(v + eps)
def _scatter_add(tgt, idx, src, axis=0):
    return tgt.clone().index_add_(axis, idx, src)
def _segment_sum(src, seg, n):
    out = torch.zeros((n,) + src.shape[1:], dtype=src.dtype)
    return out.index_add_(0, seg, src)
def _pad(x, pw):
    flat = tuple(v for pair in reversed(pw) for v in pair)
    return torch.nn.functional.pad(x, flat)

kernel_name = 'standalone__low_rank_proj_linformer__prompt'

def setup_inputs():

    _w = (lambda a: (lambda t: t.to(torch.bfloat16) if t.is_floating_point() else t)(torch.as_tensor(a)))
    B, H, Sq, Skv, Dh, R = 1, 32, 2048, 2048, 128, 8
    return {
        "q": _w((lambda *s: torch.randn(*s, dtype=torch.bfloat16) * 0.8)(B, H, Sq, Dh)),
        "k": _w((lambda *s: torch.randn(*s, dtype=torch.bfloat16) * 0.8)(B, H, Skv, Dh)),
        "v": _w((lambda *s: torch.randn(*s, dtype=torch.bfloat16) * 0.05)(B, H, Skv, Dh)),
        "e_proj": _w((lambda *s: torch.randn(*s, dtype=torch.bfloat16) * 0.8)(R, Skv) * (Skv ** -0.5)),
        "f_proj": _w((lambda *s: torch.randn(*s, dtype=torch.bfloat16) * 0.05)(R, Skv) * (Skv ** -0.5)),
    }

def reference(q, k, v, e_proj, f_proj):
    B, H, Sq, Dh = q.shape
    kp = torch.einsum("rn,bhnd->bhrd", e_proj, k)
    vp = torch.einsum("rn,bhnd->bhrd", f_proj, v)
    scores = torch.einsum("bhqd,bhrd->bhqr", q, kp) * (Dh ** -0.5)
    p = _softmax(scores, axis=-1)
    o = torch.einsum("bhqr,bhrd->bhqd", p, vp)
    return o
```

```cpp
#include <hip/hip_runtime.h>
#include <hip/hip_bf16.h>

typedef __attribute__((ext_vector_type(8))) short bf16x8;
typedef __attribute__((ext_vector_type(4))) float f32x4;
typedef __attribute__((ext_vector_type(4))) unsigned int u32x4;

constexpr int H = 32, SQ = 2048, SKV = 2048, DH = 128, R = 8;
constexpr int NSPLIT = 8;
constexpr int ROWS_PER_WG = 256;

__device__ __forceinline__ unsigned short f2bf(float f) {
    __hip_bfloat16 b = __float2bfloat16(f);
    return *reinterpret_cast<unsigned short*>(&b);
}
__device__ __forceinline__ float bfround(float f) {
    return __uint_as_float(((unsigned)f2bf(f)) << 16);
}
__device__ __forceinline__ unsigned pack2(float lo, float hi) {
    return (unsigned)f2bf(lo) | ((unsigned)f2bf(hi) << 16);
}
__device__ __forceinline__ unsigned perm_lo(unsigned a, unsigned b) {
    return __builtin_amdgcn_perm(b, a, 0x05040100u);
}
__device__ __forceinline__ unsigned perm_hi(unsigned a, unsigned b) {
    return __builtin_amdgcn_perm(b, a, 0x07060302u);
}

__global__ __launch_bounds__(1024) void proj_kernel(const unsigned short* __restrict__ k,
                                                    const unsigned short* __restrict__ v,
                                                    const unsigned short* __restrict__ e,
                                                    const unsigned short* __restrict__ f,
                                                    float* __restrict__ part) {
    __shared__ __attribute__((aligned(16))) float red[16 * R * DH];
    const int tid = threadIdx.x, lane = tid & 63, w = tid >> 6;
    const int c = lane & 15, g = lane >> 4;
    const int h = blockIdx.x >> 3, s = blockIdx.x & 7;
    const int tensor = w >> 3;
    const unsigned short* src = tensor ? v : k;
    const unsigned short* proj = tensor ? f : e;
    const int nbase = s * ROWS_PER_WG + (w & 7) * 32;

    const u32x4* p = reinterpret_cast<const u32x4*>(src + ((size_t)(h * SKV + nbase + 8 * g) * DH + 8 * c));
    u32x4 x[8];
#pragma unroll
    for (int j = 0; j < 8; ++j) x[j] = __builtin_nontemporal_load(p + j * (DH / 8));
    u32x4 a = {0u, 0u, 0u, 0u};
    if (c < R) a = *reinterpret_cast<const u32x4*>(proj + (size_t)c * SKV + nbase + 8 * g);

    f32x4 acc[8];
#pragma unroll
    for (int t = 0; t < 8; ++t) {
        u32x4 b;
#pragma unroll
        for (int q = 0; q < 4; ++q) {
            unsigned lo = x[2 * q][t >> 1], hi = x[2 * q + 1][t >> 1];
            b[q] = (t & 1) ? perm_hi(lo, hi) : perm_lo(lo, hi);
        }
        f32x4 z = {0.f, 0.f, 0.f, 0.f};
        acc[t] = __builtin_amdgcn_mfma_f32_16x16x32_bf16(__builtin_bit_cast(bf16x8, a), __builtin_bit_cast(bf16x8, b), z, 0, 0, 0);
    }
    if (g < 2) {
#pragma unroll
        for (int i = 0; i < 4; ++i) {
            float* dst = red + (w * R + 4 * g + i) * DH + 8 * c;
            f32x4 v0 = {acc[0][i], acc[1][i], acc[2][i], acc[3][i]};
            f32x4 v1 = {acc[4][i], acc[5][i], acc[6][i], acc[7][i]};
            *reinterpret_cast<f32x4*>(dst) = v0;
            *reinterpret_cast<f32x4*>(dst + 4) = v1;
        }
    }
    __syncthreads();
    if (tid < 512) {
        const int tn = tid >> 8, idx4 = tid & 255;
        f32x4 sum = {0.f, 0.f, 0.f, 0.f};
#pragma unroll
        for (int ww = 0; ww < 8; ++ww) {
            sum += *reinterpret_cast<const f32x4*>(red + ((tn * 8 + ww) * R * DH) + idx4 * 4);
        }
        *reinterpret_cast<f32x4*>(part + ((size_t)(blockIdx.x * 2 + tn) * R * DH) + idx4 * 4) = sum;
    }
}

__global__ __launch_bounds__(1024) void attn_kernel(const unsigned short* __restrict__ q,
                                                    const float* __restrict__ part,
                                                    unsigned short* __restrict__ out) {
    __shared__ __attribute__((aligned(16))) unsigned short kvp[2 * R * DH];
    const int tid = threadIdx.x, lane = tid & 63, w = tid >> 6;
    const int c = lane & 15, g = lane >> 4;
    const int h = blockIdx.x >> 3, s = blockIdx.x & 7;
    const int row0 = s * ROWS_PER_WG + w * 16;

    const u32x4* qp = reinterpret_cast<const u32x4*>(q + ((size_t)(h * SQ + row0 + c) * DH + 8 * g));
    u32x4 qf[4];
#pragma unroll
    for (int u = 0; u < 4; ++u) qf[u] = __builtin_nontemporal_load(qp + u * 4);

    if (tid < 512) {
        const int tn = tid >> 8, idx4 = tid & 255;
        f32x4 sum = {0.f, 0.f, 0.f, 0.f};
#pragma unroll
        for (int ss = 0; ss < NSPLIT; ++ss) {
            sum += *reinterpret_cast<const f32x4*>(part + ((size_t)((h * NSPLIT + ss) * 2 + tn) * R * DH) + idx4 * 4);
        }
        uint2 pk;
        pk.x = pack2(sum[0], sum[1]);
        pk.y = pack2(sum[2], sum[3]);
        *reinterpret_cast<uint2*>(kvp + tn * R * DH + idx4 * 4) = pk;
    }
    __syncthreads();

    u32x4 ka[4];
#pragma unroll
    for (int u = 0; u < 4; ++u) {
        u32x4 z = {0u, 0u, 0u, 0u};
        ka[u] = (c < R) ? *reinterpret_cast<const u32x4*>(kvp + c * DH + 32 * u + 8 * g) : z;
    }
    u32x4 vr[4];
#pragma unroll
    for (int j = 0; j < 4; ++j) {
        u32x4 z = {0u, 0u, 0u, 0u};
        vr[j] = (g < 2) ? *reinterpret_cast<const u32x4*>(kvp + R * DH + (4 * (g & 1) + j) * DH + 8 * c) : z;
    }

    f32x4 sc = {0.f, 0.f, 0.f, 0.f};
#pragma unroll
    for (int u = 0; u < 4; ++u)
        sc = __builtin_amdgcn_mfma_f32_16x16x32_bf16(__builtin_bit_cast(bf16x8, ka[u]), __builtin_bit_cast(bf16x8, qf[u]), sc, 0, 0, 0);
    const float scale = 0.08838834764831845f;
    float sv[4];
#pragma unroll
    for (int i = 0; i < 4; ++i) sv[i] = bfround(bfround(sc[i]) * scale);
    float m = fmaxf(fmaxf(sv[0], sv[1]), fmaxf(sv[2], sv[3]));
    m = fmaxf(m, __shfl_xor(m, 16));
    float ex[4];
#pragma unroll
    for (int i = 0; i < 4; ++i) ex[i] = __expf(sv[i] - m);
    float sum = (ex[0] + ex[1]) + (ex[2] + ex[3]);
    sum += __shfl_xor(sum, 16);
    const float inv = 1.0f / sum;
    u32x4 pa = {0u, 0u, 0u, 0u};
    if (g < 2) {
        pa[0] = pack2(ex[0] * inv, ex[1] * inv);
        pa[1] = pack2(ex[2] * inv, ex[3] * inv);
    }
    f32x4 o[8];
#pragma unroll
    for (int t = 0; t < 8; ++t) {
        u32x4 b = {0u, 0u, 0u, 0u};
        b[0] = (t & 1) ? perm_hi(vr[0][t >> 1], vr[1][t >> 1]) : perm_lo(vr[0][t >> 1], vr[1][t >> 1]);
        b[1] = (t & 1) ? perm_hi(vr[2][t >> 1], vr[3][t >> 1]) : perm_lo(vr[2][t >> 1], vr[3][t >> 1]);
        f32x4 z = {0.f, 0.f, 0.f, 0.f};
        o[t] = __builtin_amdgcn_mfma_f32_16x16x32_bf16(__builtin_bit_cast(bf16x8, pa), __builtin_bit_cast(bf16x8, b), z, 0, 0, 0);
    }
#pragma unroll
    for (int i = 0; i < 4; ++i) {
        u32x4 ov;
        ov[0] = pack2(o[0][i], o[1][i]);
        ov[1] = pack2(o[2][i], o[3][i]);
        ov[2] = pack2(o[4][i], o[5][i]);
        ov[3] = pack2(o[6][i], o[7][i]);
        __builtin_nontemporal_store(ov, reinterpret_cast<u32x4*>(out + ((size_t)(h * SQ + row0 + 4 * g + i) * DH + 8 * c)));
    }
}

extern "C" void kernel_launch(void* const* d_in, const int* in_sizes, int n_in,
                              void* d_out, int out_size, void* d_ws, size_t ws_size,
                              hipStream_t stream) {
    const unsigned short* q = (const unsigned short*)d_in[0];
    const unsigned short* k = (const unsigned short*)d_in[1];
    const unsigned short* v = (const unsigned short*)d_in[2];
    const unsigned short* e = (const unsigned short*)d_in[3];
    const unsigned short* f = (const unsigned short*)d_in[4];
    float* part = (float*)d_ws;
    proj_kernel<<<H * NSPLIT, 1024, 0, stream>>>(k, v, e, f, part);
    attn_kernel<<<H * 8, 1024, 0, stream>>>(q, part, (unsigned short*)d_out);
}
```

```cpp
#include <hip/hip_runtime.h>
#include <hip/hip_bf16.h>

typedef __attribute__((ext_vector_type(8))) short bf16x8;
typedef __attribute__((ext_vector_type(4))) float f32x4;
typedef __attribute__((ext_vector_type(4))) unsigned int u32x4;
typedef __attribute__((address_space(1))) unsigned gu32;
#define RLX_AGENT __ATOMIC_RELAXED, __HIP_MEMORY_SCOPE_AGENT

constexpr int H = 32, SQ = 2048, SKV = 2048, DH = 128, R = 8;
constexpr int NSPLIT = 8;
constexpr int ROWS_PER_WG = 256;
constexpr int PART_FLOATS = 2 * R * DH;
constexpr size_t PART_BYTES = (size_t)H * NSPLIT * PART_FLOATS * 4;
constexpr size_t XOFF_WORDS = (4u << 20) / 4;
constexpr size_t FOFF_WORDS = (5u << 20) / 4;
constexpr int WSTRIDE = 32;
constexpr unsigned SPIN_CAP = 1u << 22;

__device__ __forceinline__ unsigned short f2bf(float f) {
    __hip_bfloat16 b = __float2bfloat16(f);
    return *reinterpret_cast<unsigned short*>(&b);
}
__device__ __forceinline__ float bfround(float f) {
    return __uint_as_float(((unsigned)f2bf(f)) << 16);
}
__device__ __forceinline__ unsigned pack2(float lo, float hi) {
    return (unsigned)f2bf(lo) | ((unsigned)f2bf(hi) << 16);
}
__device__ __forceinline__ unsigned perm_lo(unsigned a, unsigned b) {
    return __builtin_amdgcn_perm(b, a, 0x05040100u);
}
__device__ __forceinline__ unsigned perm_hi(unsigned a, unsigned b) {
    return __builtin_amdgcn_perm(b, a, 0x07060302u);
}

__global__ __launch_bounds__(1024) void linformer_fused(const unsigned short* __restrict__ q,
                                                        const unsigned short* __restrict__ k,
                                                        const unsigned short* __restrict__ v,
                                                        const unsigned short* __restrict__ e,
                                                        const unsigned short* __restrict__ f,
                                                        unsigned short* __restrict__ out,
                                                        unsigned* __restrict__ ws) {
    __shared__ __attribute__((aligned(16))) float red[16 * R * DH];
    __shared__ __attribute__((aligned(16))) float rsum[PART_FLOATS];
    __shared__ __attribute__((aligned(16))) unsigned short kvp[2 * R * DH];
    const int tid = threadIdx.x, lane = tid & 63;
    const int w = __builtin_amdgcn_readfirstlane(tid >> 6);
    const int c = lane & 15, g = lane >> 4;
    const int b = blockIdx.x, h = b >> 3, s = b & 7;
    gu32* Xw = (gu32*)ws + XOFF_WORDS;
    gu32* Fw = (gu32*)ws + FOFF_WORDS;
    const __amdgpu_buffer_rsrc_t prsrc = __builtin_amdgcn_make_buffer_rsrc(ws, 0, (int)PART_BYTES, 0x00020000);
    const __amdgpu_buffer_rsrc_t wrsrc = __builtin_amdgcn_make_buffer_rsrc(ws, 0, 8 << 20, 0x00020000);

    gu32* a0 = (lane < 8) ? (Fw + b * WSTRIDE + lane) : (Xw + b * WSTRIDE);
    const unsigned oldv = __hip_atomic_load(a0, RLX_AGENT);

    const int tensor = w >> 3;
    const unsigned short* src = tensor ? v : k;
    const unsigned short* proj = tensor ? f : e;
    const int nbase = s * ROWS_PER_WG + (w & 7) * 32;
    const u32x4* p = reinterpret_cast<const u32x4*>(src + ((size_t)(h * SKV + nbase + 8 * g) * DH + 8 * c));
    u32x4 x[8];
    const __amdgpu_buffer_rsrc_t ersrc = __builtin_amdgcn_make_buffer_rsrc((void*)proj, 0, R * SKV * 2, 0x00020000);
    const u32x4 a = __builtin_amdgcn_raw_buffer_load_b128(ersrc, (unsigned)((c * SKV + nbase + 8 * g) * 2), 0, 0);
#pragma unroll
    for (int j = 0; j < 8; ++j) x[j] = __builtin_nontemporal_load(p + j * (DH / 8));

    const int row0 = s * ROWS_PER_WG + w * 16;
    const u32x4* qp = reinterpret_cast<const u32x4*>(q + ((size_t)(h * SQ + row0 + c) * DH + 8 * g));
    u32x4 qf[4];
#pragma unroll
    for (int u = 0; u < 4; ++u) qf[u] = __builtin_nontemporal_load(qp + u * 4);

    unsigned myx = __builtin_amdgcn_readlane(oldv, 8) + 1u;
    while (__any(oldv == myx)) ++myx;
    __builtin_amdgcn_raw_buffer_store_b32(myx, wrsrc, tid == 0 ? (unsigned)((XOFF_WORDS + b * WSTRIDE) * 4) : 0xFFFFFFF0u, 0, 16);
    __builtin_amdgcn_sched_barrier(0);

    f32x4 acc[8];
#pragma unroll
    for (int t = 0; t < 8; ++t) {
        u32x4 bb;
#pragma unroll
        for (int qq = 0; qq < 4; ++qq) {
            unsigned lo = x[2 * qq][t >> 1], hi = x[2 * qq + 1][t >> 1];
            bb[qq] = (t & 1) ? perm_hi(lo, hi) : perm_lo(lo, hi);
        }
        f32x4 z = {0.f, 0.f, 0.f, 0.f};
        acc[t] = __builtin_amdgcn_mfma_f32_16x16x32_bf16(__builtin_bit_cast(bf16x8, a), __builtin_bit_cast(bf16x8, bb), z, 0, 0, 0);
    }
    if (g < 2) {
#pragma unroll
        for (int i = 0; i < 4; ++i) {
            float* dst = red + (w * R + 4 * g + i) * DH + 8 * c;
            f32x4 v0 = {acc[0][i], acc[1][i], acc[2][i], acc[3][i]};
            f32x4 v1 = {acc[4][i], acc[5][i], acc[6][i], acc[7][i]};
            *reinterpret_cast<f32x4*>(dst) = v0;
            *reinterpret_cast<f32x4*>(dst + 4) = v1;
        }
    }
    __syncthreads();
    if (tid < 512) {
        const int tn = tid >> 8, idx4 = tid & 255;
        f32x4 sum = {0.f, 0.f, 0.f, 0.f};
#pragma unroll
        for (int ww = 0; ww < 8; ++ww)
            sum += *reinterpret_cast<const f32x4*>(red + ((tn * 8 + ww) * R * DH) + idx4 * 4);
        *reinterpret_cast<f32x4*>(rsum + tid * 4) = sum;
    }
    __syncthreads();

    if (w == 0) {
#pragma unroll
        for (int kk = 0; kk < 8; ++kk) {
            f32x4 vv = *reinterpret_cast<const f32x4*>(rsum + (kk * 64 + lane) * 4);
            __builtin_amdgcn_raw_buffer_store_b128(__builtin_bit_cast(u32x4, vv), prsrc,
                                                   (unsigned)((b * PART_FLOATS + (kk * 64 + lane) * 4) * 4), 0, 16);
        }
        gu32* pa = (lane < 8) ? (Fw + b * WSTRIDE + lane) : (Xw + (h * NSPLIT + (lane & 7)) * WSTRIDE);
        gu32* ea = Fw + (h * NSPLIT + (lane & 7)) * WSTRIDE + s;
        const bool echo_lane = (lane >= 8) && (lane < 16) && ((lane & 7) != s);
        unsigned last = 0;
        bool first = true, prev_done = false;
        for (unsigned spins = 0;; ++spins) {
            unsigned val = __hip_atomic_load(pa, RLX_AGENT);
            asm volatile("s_waitcnt vmcnt(0)" ::: "memory");
            if (echo_lane && (first || val != last)) __hip_atomic_store(ea, val, RLX_AGENT);
            last = val;
            first = false;
            if (prev_done) break;
            bool done = __all((lane >= 8) || (lane == s) || (val == myx));
            if (spins >= SPIN_CAP) done = true;
            if (done) {
                __syncthreads();
                prev_done = true;
            } else {
                __builtin_amdgcn_s_sleep(2);
            }
        }
    } else {
        __syncthreads();
        if (w >= 8) {
            const int t2 = tid - 512, tn = t2 >> 8, idx4 = t2 & 255;
            f32x4 pv[NSPLIT];
#pragma unroll
            for (int ss = 0; ss < NSPLIT; ++ss)
                pv[ss] = __builtin_bit_cast(f32x4, __builtin_amdgcn_raw_buffer_load_b128(
                    prsrc, (unsigned)(((h * NSPLIT + ss) * PART_FLOATS + t2 * 4) * 4), 0, 16));
            f32x4 sum = pv[0];
#pragma unroll
            for (int ss = 1; ss < NSPLIT; ++ss) sum += pv[ss];
            uint2 pk;
            pk.x = pack2(sum[0], sum[1]);
            pk.y = pack2(sum[2], sum[3]);
            *reinterpret_cast<uint2*>(kvp + tn * R * DH + idx4 * 4) = pk;
        }
    }
    __syncthreads();

    u32x4 ka[4];
#pragma unroll
    for (int u = 0; u < 4; ++u) {
        u32x4 z = {0u, 0u, 0u, 0u};
        ka[u] = (c < R) ? *reinterpret_cast<const u32x4*>(kvp + c * DH + 32 * u + 8 * g) : z;
    }
    u32x4 vr[4];
#pragma unroll
    for (int j = 0; j < 4; ++j) {
        u32x4 z = {0u, 0u, 0u, 0u};
        vr[j] = (g < 2) ? *reinterpret_cast<const u32x4*>(kvp + R * DH + (4 * (g & 1) + j) * DH + 8 * c) : z;
    }

    f32x4 sc = {0.f, 0.f, 0.f, 0.f};
#pragma unroll
    for (int u = 0; u < 4; ++u)
        sc = __builtin_amdgcn_mfma_f32_16x16x32_bf16(__builtin_bit_cast(bf16x8, ka[u]), __builtin_bit_cast(bf16x8, qf[u]), sc, 0, 0, 0);
    const float scale = 0.08838834764831845f;
    float sv[4];
#pragma unroll
    for (int i = 0; i < 4; ++i) sv[i] = bfround(bfround(sc[i]) * scale);
    float m = fmaxf(fmaxf(sv[0], sv[1]), fmaxf(sv[2], sv[3]));
    m = fmaxf(m, __shfl_xor(m, 16));
    float ex[4];
#pragma unroll
    for (int i = 0; i < 4; ++i) ex[i] = __expf(sv[i] - m);
    float sum = (ex[0] + ex[1]) + (ex[2] + ex[3]);
    sum += __shfl_xor(sum, 16);
    const float inv = 1.0f / sum;
    u32x4 pa = {0u, 0u, 0u, 0u};
    if (g < 2) {
        pa[0] = pack2(ex[0] * inv, ex[1] * inv);
        pa[1] = pack2(ex[2] * inv, ex[3] * inv);
    }
    f32x4 o[8];
#pragma unroll
    for (int t = 0; t < 8; ++t) {
        u32x4 bb = {0u, 0u, 0u, 0u};
        bb[0] = (t & 1) ? perm_hi(vr[0][t >> 1], vr[1][t >> 1]) : perm_lo(vr[0][t >> 1], vr[1][t >> 1]);
        bb[1] = (t & 1) ? perm_hi(vr[2][t >> 1], vr[3][t >> 1]) : perm_lo(vr[2][t >> 1], vr[3][t >> 1]);
        f32x4 z = {0.f, 0.f, 0.f, 0.f};
        o[t] = __builtin_amdgcn_mfma_f32_16x16x32_bf16(__builtin_bit_cast(bf16x8, pa), __builtin_bit_cast(bf16x8, bb), z, 0, 0, 0);
    }
#pragma unroll
    for (int i = 0; i < 4; ++i) {
        u32x4 ov;
        ov[0] = pack2(o[0][i], o[1][i]);
        ov[1] = pack2(o[2][i], o[3][i]);
        ov[2] = pack2(o[4][i], o[5][i]);
        ov[3] = pack2(o[6][i], o[7][i]);
        __builtin_nontemporal_store(ov, reinterpret_cast<u32x4*>(out + ((size_t)(h * SQ + row0 + 4 * g + i) * DH + 8 * c)));
    }
}

extern "C" void kernel_launch(void* const* d_in, const int* in_sizes, int n_in,
                              void* d_out, int out_size, void* d_ws, size_t ws_size,
                              hipStream_t stream) {
    const unsigned short* q = (const unsigned short*)d_in[0];
    const unsigned short* k = (const unsigned short*)d_in[1];
    const unsigned short* v = (const unsigned short*)d_in[2];
    const unsigned short* e = (const unsigned short*)d_in[3];
    const unsigned short* f = (const unsigned short*)d_in[4];
    linformer_fused<<<H * NSPLIT, 1024, 16384, stream>>>(q, k, v, e, f, (unsigned short*)d_out, (unsigned*)d_ws);
}
```

```cpp
#include <hip/hip_runtime.h>
#include <hip/hip_bf16.h>

typedef __attribute__((ext_vector_type(8))) short bf16x8;
typedef __attribute__((ext_vector_type(4))) float f32x4;
typedef __attribute__((ext_vector_type(4))) unsigned int u32x4;
typedef __attribute__((address_space(1))) unsigned gu32;
#define RLX_AGENT __ATOMIC_RELAXED, __HIP_MEMORY_SCOPE_AGENT

constexpr int H = 32, SQ = 2048, SKV = 2048, DH = 128, R = 8;
constexpr int NSPLIT = 8;
constexpr int ROWS_PER_WG = 256;
constexpr int PART_FLOATS = R * DH;
constexpr size_t PART_BYTES = (size_t)H * NSPLIT * PART_FLOATS * 4;
constexpr size_t XOFF_WORDS = (4u << 20) / 4;
constexpr size_t FOFF_WORDS = (5u << 20) / 4;
constexpr int WSTRIDE = 32;
constexpr unsigned SPIN_CAP = 1u << 22;

__device__ __forceinline__ unsigned short f2bf(float f) {
    __hip_bfloat16 b = __float2bfloat16(f);
    return *reinterpret_cast<unsigned short*>(&b);
}
__device__ __forceinline__ float bfround(float f) {
    return __uint_as_float(((unsigned)f2bf(f)) << 16);
}
__device__ __forceinline__ unsigned pack2(float lo, float hi) {
    return (unsigned)f2bf(lo) | ((unsigned)f2bf(hi) << 16);
}
__device__ __forceinline__ unsigned perm_lo(unsigned a, unsigned b) {
    return __builtin_amdgcn_perm(b, a, 0x05040100u);
}
__device__ __forceinline__ unsigned perm_hi(unsigned a, unsigned b) {
    return __builtin_amdgcn_perm(b, a, 0x07060302u);
}

__global__ __launch_bounds__(1024) void linformer_fused(const unsigned short* __restrict__ q,
                                                        const unsigned short* __restrict__ k,
                                                        const unsigned short* __restrict__ v,
                                                        const unsigned short* __restrict__ e,
                                                        const unsigned short* __restrict__ f,
                                                        unsigned short* __restrict__ out,
                                                        unsigned* __restrict__ ws) {
    __shared__ __attribute__((aligned(16))) float red[16 * R * DH];
    __shared__ __attribute__((aligned(16))) float rsum[PART_FLOATS];
    __shared__ __attribute__((aligned(16))) unsigned short kvp[2 * R * DH];
    const int tid = threadIdx.x, lane = tid & 63;
    const int w = __builtin_amdgcn_readfirstlane(tid >> 6);
    const int c = lane & 15, g = lane >> 4;
    const int b = blockIdx.x, h = b >> 3, s = b & 7;
    gu32* Xw = (gu32*)ws + XOFF_WORDS;
    gu32* Fw = (gu32*)ws + FOFF_WORDS;
    const __amdgpu_buffer_rsrc_t prsrc = __builtin_amdgcn_make_buffer_rsrc(ws, 0, (int)PART_BYTES, 0x00020000);
    const __amdgpu_buffer_rsrc_t wrsrc = __builtin_amdgcn_make_buffer_rsrc(ws, 0, 8 << 20, 0x00020000);

    gu32* a0 = (lane < 8) ? (Fw + b * WSTRIDE + lane) : (Xw + b * WSTRIDE);
    const unsigned oldv = __hip_atomic_load(a0, RLX_AGENT);

    const int tensor = s >> 2;
    const unsigned short* src = tensor ? v : k;
    const unsigned short* proj = tensor ? f : e;
    const int nbase = (s & 3) * 512 + w * 32;
    const u32x4* p = reinterpret_cast<const u32x4*>(src + ((size_t)(h * SKV + nbase + 8 * g) * DH + 8 * c));
    u32x4 x[8];
    const __amdgpu_buffer_rsrc_t ersrc = __builtin_amdgcn_make_buffer_rsrc((void*)proj, 0, R * SKV * 2, 0x00020000);
    const u32x4 a = __builtin_amdgcn_raw_buffer_load_b128(ersrc, (unsigned)((c * SKV + nbase + 8 * g) * 2), 0, 0);
#pragma unroll
    for (int j = 0; j < 8; ++j) x[j] = __builtin_nontemporal_load(p + j * (DH / 8));

    const int row0 = s * ROWS_PER_WG + w * 16;
    const u32x4* qp = reinterpret_cast<const u32x4*>(q + ((size_t)(h * SQ + row0 + c) * DH + 8 * g));
    u32x4 qf[4];
#pragma unroll
    for (int u = 0; u < 4; ++u) qf[u] = __builtin_nontemporal_load(qp + u * 4);

    unsigned myx = __builtin_amdgcn_readlane(oldv, 8) + 1u;
    while (__any(oldv == myx)) ++myx;
    __builtin_amdgcn_raw_buffer_store_b32(myx, wrsrc, tid == 0 ? (unsigned)((XOFF_WORDS + b * WSTRIDE) * 4) : 0xFFFFFFF0u, 0, 16);
    __builtin_amdgcn_sched_barrier(0);

    f32x4 acc[8];
#pragma unroll
    for (int t = 0; t < 8; ++t) {
        u32x4 bb;
#pragma unroll
        for (int qq = 0; qq < 4; ++qq) {
            unsigned lo = x[2 * qq][t >> 1], hi = x[2 * qq + 1][t >> 1];
            bb[qq] = (t & 1) ? perm_hi(lo, hi) : perm_lo(lo, hi);
        }
        f32x4 z = {0.f, 0.f, 0.f, 0.f};
        acc[t] = __builtin_amdgcn_mfma_f32_16x16x32_bf16(__builtin_bit_cast(bf16x8, a), __builtin_bit_cast(bf16x8, bb), z, 0, 0, 0);
    }
    if (g < 2) {
#pragma unroll
        for (int i = 0; i < 4; ++i) {
            float* dst = red + (w * R + 4 * g + i) * DH + 8 * c;
            f32x4 v0 = {acc[0][i], acc[1][i], acc[2][i], acc[3][i]};
            f32x4 v1 = {acc[4][i], acc[5][i], acc[6][i], acc[7][i]};
            *reinterpret_cast<f32x4*>(dst) = v0;
            *reinterpret_cast<f32x4*>(dst + 4) = v1;
        }
    }
    __syncthreads();
    if (tid < 256) {
        f32x4 sum = {0.f, 0.f, 0.f, 0.f};
#pragma unroll
        for (int ww = 0; ww < 16; ++ww)
            sum += *reinterpret_cast<const f32x4*>(red + (ww * R * DH) + tid * 4);
        *reinterpret_cast<f32x4*>(rsum + tid * 4) = sum;
    }
    __syncthreads();

    if (w == 0) {
#pragma unroll
        for (int kk = 0; kk < 4; ++kk) {
            f32x4 vv = *reinterpret_cast<const f32x4*>(rsum + (kk * 64 + lane) * 4);
            __builtin_amdgcn_raw_buffer_store_b128(__builtin_bit_cast(u32x4, vv), prsrc,
                                                   (unsigned)((b * PART_FLOATS + (kk * 64 + lane) * 4) * 4), 0, 16);
        }
        gu32* pa = (lane < 8) ? (Fw + b * WSTRIDE + lane) : (Xw + (h * NSPLIT + (lane & 7)) * WSTRIDE);
        gu32* ea = Fw + (h * NSPLIT + (lane & 7)) * WSTRIDE + s;
        const bool echo_lane = (lane >= 8) && (lane < 16) && ((lane & 7) != s);
        unsigned last = 0;
        bool first = true, prev_done = false;
        for (unsigned spins = 0;; ++spins) {
            unsigned val = __hip_atomic_load(pa, RLX_AGENT);
            asm volatile("s_waitcnt vmcnt(0)" ::: "memory");
            if (echo_lane && (first || val != last)) __hip_atomic_store(ea, val, RLX_AGENT);
            last = val;
            first = false;
            if (prev_done) break;
            bool done = __all((lane >= 8) || (lane == s) || (val == myx));
            if (spins >= SPIN_CAP) done = true;
            if (done) {
                __syncthreads();
                prev_done = true;
            } else {
                __builtin_amdgcn_s_sleep(1);
            }
        }
    } else {
        __syncthreads();
        if (w >= 8) {
            const int t2 = tid - 512, tn = t2 >> 8, idx4 = t2 & 255;
            f32x4 pv[4];
#pragma unroll
            for (int ss = 0; ss < 4; ++ss)
                pv[ss] = __builtin_bit_cast(f32x4, __builtin_amdgcn_raw_buffer_load_b128(
                    prsrc, (unsigned)(((h * NSPLIT + tn * 4 + ss) * PART_FLOATS + idx4 * 4) * 4), 0, 16));
            f32x4 sum = pv[0];
#pragma unroll
            for (int ss = 1; ss < 4; ++ss) sum += pv[ss];
            uint2 pk;
            pk.x = pack2(sum[0], sum[1]);
            pk.y = pack2(sum[2], sum[3]);
            *reinterpret_cast<uint2*>(kvp + tn * R * DH + idx4 * 4) = pk;
        }
    }
    __syncthreads();

    u32x4 ka[4];
#pragma unroll
    for (int u = 0; u < 4; ++u) {
        u32x4 z = {0u, 0u, 0u, 0u};
        ka[u] = (c < R) ? *reinterpret_cast<const u32x4*>(kvp + c * DH + 32 * u + 8 * g) : z;
    }
    u32x4 vr[4];
#pragma unroll
    for (int j = 0; j < 4; ++j) {
        u32x4 z = {0u, 0u, 0u, 0u};
        vr[j] = (g < 2) ? *reinterpret_cast<const u32x4*>(kvp + R * DH + (4 * (g & 1) + j) * DH + 8 * c) : z;
    }

    f32x4 sc = {0.f, 0.f, 0.f, 0.f};
#pragma unroll
    for (int u = 0; u < 4; ++u)
        sc = __builtin_amdgcn_mfma_f32_16x16x32_bf16(__builtin_bit_cast(bf16x8, ka[u]), __builtin_bit_cast(bf16x8, qf[u]), sc, 0, 0, 0);
    const float scale = 0.08838834764831845f;
    float sv[4];
#pragma unroll
    for (int i = 0; i < 4; ++i) sv[i] = bfround(bfround(sc[i]) * scale);
    float m = fmaxf(fmaxf(sv[0], sv[1]), fmaxf(sv[2], sv[3]));
    m = fmaxf(m, __shfl_xor(m, 16));
    float ex[4];
#pragma unroll
    for (int i = 0; i < 4; ++i) ex[i] = __expf(sv[i] - m);
    float sum = (ex[0] + ex[1]) + (ex[2] + ex[3]);
    sum += __shfl_xor(sum, 16);
    const float inv = 1.0f / sum;
    u32x4 pa = {0u, 0u, 0u, 0u};
    if (g < 2) {
        pa[0] = pack2(ex[0] * inv, ex[1] * inv);
        pa[1] = pack2(ex[2] * inv, ex[3] * inv);
    }
    f32x4 o[8];
#pragma unroll
    for (int t = 0; t < 8; ++t) {
        u32x4 bb = {0u, 0u, 0u, 0u};
        bb[0] = (t & 1) ? perm_hi(vr[0][t >> 1], vr[1][t >> 1]) : perm_lo(vr[0][t >> 1], vr[1][t >> 1]);
        bb[1] = (t & 1) ? perm_hi(vr[2][t >> 1], vr[3][t >> 1]) : perm_lo(vr[2][t >> 1], vr[3][t >> 1]);
        f32x4 z = {0.f, 0.f, 0.f, 0.f};
        o[t] = __builtin_amdgcn_mfma_f32_16x16x32_bf16(__builtin_bit_cast(bf16x8, pa), __builtin_bit_cast(bf16x8, bb), z, 0, 0, 0);
    }
#pragma unroll
    for (int i = 0; i < 4; ++i) {
        u32x4 ov;
        ov[0] = pack2(o[0][i], o[1][i]);
        ov[1] = pack2(o[2][i], o[3][i]);
        ov[2] = pack2(o[4][i], o[5][i]);
        ov[3] = pack2(o[6][i], o[7][i]);
        __builtin_nontemporal_store(ov, reinterpret_cast<u32x4*>(out + ((size_t)(h * SQ + row0 + 4 * g + i) * DH + 8 * c)));
    }
}

extern "C" void kernel_launch(void* const* d_in, const int* in_sizes, int n_in,
                              void* d_out, int out_size, void* d_ws, size_t ws_size,
                              hipStream_t stream) {
    const unsigned short* q = (const unsigned short*)d_in[0];
    const unsigned short* k = (const unsigned short*)d_in[1];
    const unsigned short* v = (const unsigned short*)d_in[2];
    const unsigned short* e = (const unsigned short*)d_in[3];
    const unsigned short* f = (const unsigned short*)d_in[4];
    linformer_fused<<<H * NSPLIT, 1024, 16384, stream>>>(q, k, v, e, f, (unsigned short*)d_out, (unsigned*)d_ws);
}
```

```cpp
#include <hip/hip_runtime.h>
#include <hip/hip_bf16.h>

typedef __attribute__((ext_vector_type(8))) short bf16x8;
typedef __attribute__((ext_vector_type(4))) float f32x4;
typedef __attribute__((ext_vector_type(4))) unsigned int u32x4;
typedef __attribute__((address_space(1))) unsigned gu32;
#define RLX_AGENT __ATOMIC_RELAXED, __HIP_MEMORY_SCOPE_AGENT

constexpr int H = 32, SQ = 2048, SKV = 2048, DH = 128, R = 8;
constexpr int NSPLIT = 8;
constexpr int ROWS_PER_WG = 256;
constexpr int PART_FLOATS = R * DH;
constexpr size_t PART_BYTES = (size_t)H * NSPLIT * PART_FLOATS * 4;
constexpr size_t XOFF_WORDS = (4u << 20) / 4;
constexpr size_t FOFF_WORDS = (5u << 20) / 4;
constexpr int WSTRIDE = 32;
constexpr unsigned SPIN_CAP = 1u << 22;

__device__ __forceinline__ unsigned short f2bf(float f) {
    __hip_bfloat16 b = __float2bfloat16(f);
    return *reinterpret_cast<unsigned short*>(&b);
}
__device__ __forceinline__ float bfround(float f) {
    return __uint_as_float(((unsigned)f2bf(f)) << 16);
}
__device__ __forceinline__ unsigned pack2(float lo, float hi) {
    return (unsigned)f2bf(lo) | ((unsigned)f2bf(hi) << 16);
}
__device__ __forceinline__ unsigned perm_lo(unsigned a, unsigned b) {
    return __builtin_amdgcn_perm(b, a, 0x05040100u);
}
__device__ __forceinline__ unsigned perm_hi(unsigned a, unsigned b) {
    return __builtin_amdgcn_perm(b, a, 0x07060302u);
}
__device__ __forceinline__ void lds_wait_ge(unsigned* p, unsigned want) {
    for (unsigned spins = 0; spins < SPIN_CAP; ++spins) {
        if (__builtin_amdgcn_readfirstlane(__hip_atomic_load(p, __ATOMIC_ACQUIRE, __HIP_MEMORY_SCOPE_WORKGROUP)) >= want) return;
        __builtin_amdgcn_s_sleep(1);
    }
}

#define STAMP(i)
#define STAMPW(i, ww)

__global__ __launch_bounds__(1024) void linformer_fused(const unsigned short* __restrict__ q,
                                                        const unsigned short* __restrict__ k,
                                                        const unsigned short* __restrict__ v,
                                                        const unsigned short* __restrict__ e,
                                                        const unsigned short* __restrict__ f,
                                                        unsigned short* __restrict__ out,
                                                        unsigned* __restrict__ ws) {
    __shared__ __attribute__((aligned(16))) float red[8 * R * DH];
    __shared__ __attribute__((aligned(16))) unsigned short kvp[2 * R * DH];
    __shared__ unsigned cnt[4];
    __shared__ u32x4 qs[8 * 4 * 64];
    const int tid = threadIdx.x, lane = tid & 63;
    const int w = __builtin_amdgcn_readfirstlane(tid >> 6);
    const int c = lane & 15, g = lane >> 4;
    const int b = blockIdx.x, h = b >> 3, s = b & 7;
    STAMP(0);
    if (tid < 4) cnt[tid] = 0u;
    u32x4 qf[4];

    if (w < 8) {
        gu32* Xw = (gu32*)ws + XOFF_WORDS;
        gu32* Fw = (gu32*)ws + FOFF_WORDS;
        const __amdgpu_buffer_rsrc_t prsrc = __builtin_amdgcn_make_buffer_rsrc(ws, 0, (int)PART_BYTES, 0x00020000);
        const __amdgpu_buffer_rsrc_t wrsrc = __builtin_amdgcn_make_buffer_rsrc(ws, 0, 8 << 20, 0x00020000);
        gu32* a0 = (lane < 8) ? (Fw + b * WSTRIDE + lane) : (Xw + b * WSTRIDE);
        const unsigned oldv = __hip_atomic_load(a0, RLX_AGENT);

        const int tensor = s >> 2;
        const unsigned short* src = tensor ? v : k;
        const unsigned short* proj = tensor ? f : e;
        const int nbase = (s & 3) * 512 + w * 64;
        const __amdgpu_buffer_rsrc_t ersrc = __builtin_amdgcn_make_buffer_rsrc((void*)proj, 0, R * SKV * 2, 0x00020000);
        u32x4 a[2];
#pragma unroll
        for (int st = 0; st < 2; ++st)
            a[st] = __builtin_amdgcn_raw_buffer_load_b128(ersrc, (unsigned)((c * SKV + nbase + 32 * st + 8 * g) * 2), 0, 0);
        const u32x4* p = reinterpret_cast<const u32x4*>(src + ((size_t)(h * SKV + nbase + 8 * g) * DH + 8 * c));
        u32x4 x[16];
#pragma unroll
        for (int j = 0; j < 16; ++j) x[j] = __builtin_nontemporal_load(p + ((j >> 3) * 32 + (j & 7)) * (DH / 8));

        unsigned myx = __builtin_amdgcn_readlane(oldv, 8) + 1u;
        while (__any(oldv == myx)) ++myx;
        __builtin_amdgcn_raw_buffer_store_b32(myx, wrsrc, tid == 0 ? (unsigned)((XOFF_WORDS + b * WSTRIDE) * 4) : 0xFFFFFFF0u, 0, 16);
        __builtin_amdgcn_sched_barrier(0);
        __syncthreads();
        __builtin_amdgcn_sched_barrier(0);

        f32x4 acc[8];
#pragma unroll
        for (int t = 0; t < 8; ++t) acc[t] = (f32x4){0.f, 0.f, 0.f, 0.f};
#pragma unroll
        for (int st = 0; st < 2; ++st) {
#pragma unroll
            for (int t = 0; t < 8; ++t) {
                u32x4 bb;
#pragma unroll
                for (int qq = 0; qq < 4; ++qq) {
                    unsigned lo = x[8 * st + 2 * qq][t >> 1], hi = x[8 * st + 2 * qq + 1][t >> 1];
                    bb[qq] = (t & 1) ? perm_hi(lo, hi) : perm_lo(lo, hi);
                }
                acc[t] = __builtin_amdgcn_mfma_f32_16x16x32_bf16(__builtin_bit_cast(bf16x8, a[st]), __builtin_bit_cast(bf16x8, bb), acc[t], 0, 0, 0);
            }
        }
        if (g < 2) {
#pragma unroll
            for (int i = 0; i < 4; ++i) {
                float* dst = red + (w * R + 4 * g + i) * DH + 8 * c;
                f32x4 v0 = {acc[0][i], acc[1][i], acc[2][i], acc[3][i]};
                f32x4 v1 = {acc[4][i], acc[5][i], acc[6][i], acc[7][i]};
                *reinterpret_cast<f32x4*>(dst) = v0;
                *reinterpret_cast<f32x4*>(dst + 4) = v1;
            }
        }
        STAMP(1);
        if (lane == 0) __hip_atomic_fetch_add(&cnt[0], 1u, __ATOMIC_RELEASE, __HIP_MEMORY_SCOPE_WORKGROUP);
        lds_wait_ge(&cnt[0], 8u);
        STAMP(2);
        if (w < 4) {
            f32x4 sum = *reinterpret_cast<const f32x4*>(red + tid * 4);
#pragma unroll
            for (int ww = 1; ww < 8; ++ww)
                sum += *reinterpret_cast<const f32x4*>(red + (ww * R * DH) + tid * 4);
            __builtin_amdgcn_raw_buffer_store_b128(__builtin_bit_cast(u32x4, sum), prsrc,
                                                   (unsigned)((b * PART_FLOATS + tid * 4) * 4), 0, 16);
        }
        gu32* pa = (lane < 8) ? (Fw + b * WSTRIDE + lane) : (Xw + (h * NSPLIT + (lane & 7)) * WSTRIDE);
        unsigned val = 0;
        if (w == 0) val = __hip_atomic_load(pa, RLX_AGENT);
        asm volatile("s_waitcnt vmcnt(0)" ::: "memory");
        if (lane == 0) __hip_atomic_fetch_add(&cnt[1], 1u, __ATOMIC_RELEASE, __HIP_MEMORY_SCOPE_WORKGROUP);
        if (w == 0) {
            lds_wait_ge(&cnt[1], 8u);
            STAMP(3);
            gu32* ea = Fw + (h * NSPLIT + (lane & 7)) * WSTRIDE + s;
            const bool echo_lane = (lane >= 8) && (lane < 16) && ((lane & 7) != s);
            if (echo_lane) __hip_atomic_store(ea, val, RLX_AGENT);
            unsigned last = val;
            for (unsigned spins = 0;; ++spins) {
                bool done = __all((lane >= 8) || (lane == s) || (val == myx));
                if (spins >= SPIN_CAP) done = true;
                if (done) break;
                __builtin_amdgcn_s_sleep(1);
                val = __hip_atomic_load(pa, RLX_AGENT);
                asm volatile("s_waitcnt vmcnt(0)" ::: "memory");
                if (echo_lane && val != last) __hip_atomic_store(ea, val, RLX_AGENT);
                last = val;
            }
            STAMP(4);
            if (lane == 0) __hip_atomic_store(&cnt[2], 1u, __ATOMIC_RELEASE, __HIP_MEMORY_SCOPE_WORKGROUP);
            val = __hip_atomic_load(pa, RLX_AGENT);
            asm volatile("s_waitcnt vmcnt(0)" ::: "memory");
            if (echo_lane && val != last) __hip_atomic_store(ea, val, RLX_AGENT);
        } else {
            lds_wait_ge(&cnt[2], 1u);
        }
        {
            const int tn = tid >> 8, idx4 = tid & 255;
            f32x4 pv[4];
#pragma unroll
            for (int ss = 0; ss < 4; ++ss)
                pv[ss] = __builtin_bit_cast(f32x4, __builtin_amdgcn_raw_buffer_load_b128(
                    prsrc, (unsigned)(((h * NSPLIT + tn * 4 + ss) * PART_FLOATS + idx4 * 4) * 4), 0, 16));
            f32x4 sum = pv[0];
#pragma unroll
            for (int ss = 1; ss < 4; ++ss) sum += pv[ss];
            uint2 pk;
            pk.x = pack2(sum[0], sum[1]);
            pk.y = pack2(sum[2], sum[3]);
            *reinterpret_cast<uint2*>(kvp + tn * R * DH + idx4 * 4) = pk;
        }
        STAMPW(5, 1);
    } else {
        __syncthreads();
        const int rq = s * ROWS_PER_WG + (w - 8) * 32;
        const u32x4* qp = reinterpret_cast<const u32x4*>(q + ((size_t)(h * SQ + rq + c) * DH + 8 * g));
        u32x4 q1[4];
#pragma unroll
        for (int u = 0; u < 4; ++u) q1[u] = __builtin_nontemporal_load(qp + 16 * (DH / 8) + u * 4);
#pragma unroll
        for (int u = 0; u < 4; ++u) qf[u] = __builtin_nontemporal_load(qp + u * 4);
#pragma unroll
        for (int u = 0; u < 4; ++u) qs[((w - 8) * 4 + u) * 64 + lane] = q1[u];
        STAMPW(6, 15);
    }
    __syncthreads();
    STAMPW(7, 15);
    const int row0 = s * ROWS_PER_WG + (w & 7) * 32 + (w < 8 ? 16 : 0);
    if (w < 8) {
#pragma unroll
        for (int u = 0; u < 4; ++u) qf[u] = qs[(w * 4 + u) * 64 + lane];
    }

    u32x4 ka[4];
#pragma unroll
    for (int u = 0; u < 4; ++u) {
        u32x4 z = {0u, 0u, 0u, 0u};
        ka[u] = (c < R) ? *reinterpret_cast<const u32x4*>(kvp + c * DH + 32 * u + 8 * g) : z;
    }
    u32x4 vr[4];
#pragma unroll
    for (int j = 0; j < 4; ++j) {
        u32x4 z = {0u, 0u, 0u, 0u};
        vr[j] = (g < 2) ? *reinterpret_cast<const u32x4*>(kvp + R * DH + (4 * (g & 1) + j) * DH + 8 * c) : z;
    }
    const float scale = 0.08838834764831845f;
    {
        f32x4 sc = {0.f, 0.f, 0.f, 0.f};
#pragma unroll
        for (int u = 0; u < 4; ++u)
            sc = __builtin_amdgcn_mfma_f32_16x16x32_bf16(__builtin_bit_cast(bf16x8, ka[u]), __builtin_bit_cast(bf16x8, qf[u]), sc, 0, 0, 0);
        float sv[4];
#pragma unroll
        for (int i = 0; i < 4; ++i) sv[i] = bfround(bfround(sc[i]) * scale);
        float m = fmaxf(fmaxf(sv[0], sv[1]), fmaxf(sv[2], sv[3]));
        m = fmaxf(m, __shfl_xor(m, 16));
        float ex[4];
#pragma unroll
        for (int i = 0; i < 4; ++i) ex[i] = __expf(sv[i] - m);
        float sum = (ex[0] + ex[1]) + (ex[2] + ex[3]);
        sum += __shfl_xor(sum, 16);
        const float inv = 1.0f / sum;
        u32x4 pa = {0u, 0u, 0u, 0u};
        if (g < 2) {
            pa[0] = pack2(ex[0] * inv, ex[1] * inv);
            pa[1] = pack2(ex[2] * inv, ex[3] * inv);
        }
        f32x4 o[8];
#pragma unroll
        for (int t = 0; t < 8; ++t) {
            u32x4 bb = {0u, 0u, 0u, 0u};
            bb[0] = (t & 1) ? perm_hi(vr[0][t >> 1], vr[1][t >> 1]) : perm_lo(vr[0][t >> 1], vr[1][t >> 1]);
            bb[1] = (t & 1) ? perm_hi(vr[2][t >> 1], vr[3][t >> 1]) : perm_lo(vr[2][t >> 1], vr[3][t >> 1]);
            f32x4 z = {0.f, 0.f, 0.f, 0.f};
            o[t] = __builtin_amdgcn_mfma_f32_16x16x32_bf16(__builtin_bit_cast(bf16x8, pa), __builtin_bit_cast(bf16x8, bb), z, 0, 0, 0);
        }
#pragma unroll
        for (int i = 0; i < 4; ++i) {
            u32x4 ov;
            ov[0] = pack2(o[0][i], o[1][i]);
            ov[1] = pack2(o[2][i], o[3][i]);
            ov[2] = pack2(o[4][i], o[5][i]);
            ov[3] = pack2(o[6][i], o[7][i]);
            __builtin_nontemporal_store(ov, reinterpret_cast<u32x4*>(out + ((size_t)(h * SQ + row0 + 4 * g + i) * DH + 8 * c)));
        }
    }
    STAMPW(8, 15);
    STAMPW(9, 15);
}

extern "C" void kernel_launch(void* const* d_in, const int* in_sizes, int n_in,
                              void* d_out, int out_size, void* d_ws, size_t ws_size,
                              hipStream_t stream) {
    const unsigned short* q = (const unsigned short*)d_in[0];
    const unsigned short* k = (const unsigned short*)d_in[1];
    const unsigned short* v = (const unsigned short*)d_in[2];
    const unsigned short* e = (const unsigned short*)d_in[3];
    const unsigned short* f = (const unsigned short*)d_in[4];
    linformer_fused<<<H * NSPLIT, 1024, 24 * 1024, stream>>>(q, k, v, e, f, (unsigned short*)d_out, (unsigned*)d_ws);
}
```

```cpp
#include <hip/hip_runtime.h>
#include <hip/hip_bf16.h>

typedef __attribute__((ext_vector_type(8))) short bf16x8;
typedef __attribute__((ext_vector_type(4))) float f32x4;
typedef __attribute__((ext_vector_type(4))) unsigned int u32x4;
typedef __attribute__((address_space(1))) unsigned gu32;
#define RLX_AGENT __ATOMIC_RELAXED, __HIP_MEMORY_SCOPE_AGENT

constexpr int H = 32, SQ = 2048, SKV = 2048, DH = 128, R = 8;
constexpr int NSPLIT = 8;
constexpr int ROWS_PER_WG = 256;
constexpr int PART_FLOATS = R * DH;
constexpr size_t PART_BYTES = (size_t)H * NSPLIT * PART_FLOATS * 4;
constexpr size_t XOFF_WORDS = (4u << 20) / 4;
constexpr size_t FOFF_WORDS = (5u << 20) / 4;
constexpr int WSTRIDE = 32;
#define NA 4
#define NB 2
constexpr unsigned SPIN_CAP = 1u << 22;

__device__ __forceinline__ unsigned short f2bf(float f) {
    __hip_bfloat16 b = __float2bfloat16(f);
    return *reinterpret_cast<unsigned short*>(&b);
}
__device__ __forceinline__ float bfround(float f) {
    return __uint_as_float(((unsigned)f2bf(f)) << 16);
}
__device__ __forceinline__ unsigned pack2(float lo, float hi) {
    return (unsigned)f2bf(lo) | ((unsigned)f2bf(hi) << 16);
}
typedef __attribute__((ext_vector_type(2))) float f32x2;
typedef __attribute__((ext_vector_type(2))) __bf16 bf16x2;
__device__ __forceinline__ unsigned cvt_pk(float a, float b) {
    f32x2 v = {a, b};
    return __builtin_bit_cast(unsigned, __builtin_convertvector(v, bf16x2));
}
__device__ __forceinline__ float lo_f(unsigned p) { return __uint_as_float(p << 16); }
__device__ __forceinline__ float hi_f(unsigned p) { return __uint_as_float(p & 0xFFFF0000u); }
__device__ __forceinline__ unsigned perm_lo(unsigned a, unsigned b) {
    return __builtin_amdgcn_perm(b, a, 0x05040100u);
}
__device__ __forceinline__ unsigned perm_hi(unsigned a, unsigned b) {
    return __builtin_amdgcn_perm(b, a, 0x07060302u);
}
__device__ __forceinline__ void lds_wait_ge(unsigned* p, unsigned want) {
#pragma unroll 1
    for (unsigned spins = 0; spins < SPIN_CAP; ++spins) {
        if (__builtin_amdgcn_readfirstlane(__hip_atomic_load(p, __ATOMIC_ACQUIRE, __HIP_MEMORY_SCOPE_WORKGROUP)) >= want) return;
        __builtin_amdgcn_s_sleep(1);
    }
}

#define STAMP(i)
#define STAMPW(i, ww)

__global__ __launch_bounds__(1024) void linformer_fused(const unsigned short* __restrict__ q,
                                                        const unsigned short* __restrict__ k,
                                                        const unsigned short* __restrict__ v,
                                                        const unsigned short* __restrict__ e,
                                                        const unsigned short* __restrict__ f,
                                                        unsigned short* __restrict__ out,
                                                        unsigned* __restrict__ ws) {
    __shared__ __attribute__((aligned(16))) float red[8 * R * DH];
    __shared__ u32x4 kaf[4 * 64];
    __shared__ u32x4 vbf[8 * 64];
    __shared__ unsigned cnt[4];
    __shared__ u32x4 qs[8 * 4 * 64];
    const int tid = threadIdx.x, lane = tid & 63;
    const int w = __builtin_amdgcn_readfirstlane(tid >> 6);
    const int c = lane & 15, g = lane >> 4;
    const int b = blockIdx.x, h = b >> 3, s = b & 7;
    STAMP(0);
    if (tid < 4) cnt[tid] = 0u;
    u32x4 qf[4];

    if (w < 8) {
        gu32* Xw = (gu32*)ws + XOFF_WORDS;
        gu32* Fw = (gu32*)ws + FOFF_WORDS;
        const __amdgpu_buffer_rsrc_t prsrc = __builtin_amdgcn_make_buffer_rsrc(ws, 0, (int)PART_BYTES, 0x00020000);
        const __amdgpu_buffer_rsrc_t wrsrc = __builtin_amdgcn_make_buffer_rsrc(ws, 0, 8 << 20, 0x00020000);
        gu32* a0 = (lane < 8) ? (Fw + b * WSTRIDE + lane) : (Xw + b * WSTRIDE);
        const unsigned oldv = __hip_atomic_load(a0, RLX_AGENT);

        const int tensor = s >> 2;
        const unsigned short* src = tensor ? v : k;
        const unsigned short* proj = tensor ? f : e;
        const int nbase = (s & 3) * 512 + w * 64;
        const __amdgpu_buffer_rsrc_t ersrc = __builtin_amdgcn_make_buffer_rsrc((void*)proj, 0, R * SKV * 2, 0x00020000);
        u32x4 a[2];
#pragma unroll
        for (int st = 0; st < 2; ++st)
            a[st] = __builtin_amdgcn_raw_buffer_load_b128(ersrc, (unsigned)((c * SKV + nbase + 32 * st + 8 * g) * 2), 0, 0);
        const u32x4* p = reinterpret_cast<const u32x4*>(src + ((size_t)(h * SKV + nbase + 8 * g) * DH + 8 * c));
        u32x4 x[16];
#pragma unroll
        for (int j = 0; j < 16; ++j) x[j] = __builtin_nontemporal_load(p + ((j >> 3) * 32 + (j & 7)) * (DH / 8));

        unsigned myx = __builtin_amdgcn_readlane(oldv, 8) + 1u;
        while (__any(oldv == myx)) ++myx;
        __builtin_amdgcn_raw_buffer_store_b32(myx, wrsrc, tid == 0 ? (unsigned)((XOFF_WORDS + b * WSTRIDE) * 4) : 0xFFFFFFF0u, 0, 16);
        __builtin_amdgcn_sched_barrier(0);
        __syncthreads();
        __builtin_amdgcn_sched_barrier(0);

        f32x4 acc[8];
#pragma unroll
        for (int t = 0; t < 8; ++t) acc[t] = (f32x4){0.f, 0.f, 0.f, 0.f};
#pragma unroll
        for (int st = 0; st < 2; ++st) {
#pragma unroll
            for (int t = 0; t < 8; ++t) {
                u32x4 bb;
#pragma unroll
                for (int qq = 0; qq < 4; ++qq) {
                    unsigned lo = x[8 * st + 2 * qq][t >> 1], hi = x[8 * st + 2 * qq + 1][t >> 1];
                    bb[qq] = (t & 1) ? perm_hi(lo, hi) : perm_lo(lo, hi);
                }
                acc[t] = __builtin_amdgcn_mfma_f32_16x16x32_bf16(__builtin_bit_cast(bf16x8, a[st]), __builtin_bit_cast(bf16x8, bb), acc[t], 0, 0, 0);
            }
        }
        if (g < 2) {
#pragma unroll
            for (int i = 0; i < 4; ++i) {
                float* dst = red + (w * R + 4 * g + i) * DH + 8 * c;
                f32x4 v0 = {acc[0][i], acc[1][i], acc[2][i], acc[3][i]};
                f32x4 v1 = {acc[4][i], acc[5][i], acc[6][i], acc[7][i]};
                *reinterpret_cast<f32x4*>(dst) = v0;
                *reinterpret_cast<f32x4*>(dst + 4) = v1;
            }
        }
        STAMP(1);
        if (lane == 0) __hip_atomic_fetch_add(&cnt[0], 1u, __ATOMIC_RELEASE, __HIP_MEMORY_SCOPE_WORKGROUP);
        lds_wait_ge(&cnt[0], 8u);
        STAMP(2);
        if (w < 4) {
            f32x4 sum = *reinterpret_cast<const f32x4*>(red + tid * 4);
#pragma unroll
            for (int ww = 1; ww < 8; ++ww)
                sum += *reinterpret_cast<const f32x4*>(red + (ww * R * DH) + tid * 4);
            __builtin_amdgcn_raw_buffer_store_b128(__builtin_bit_cast(u32x4, sum), prsrc,
                                                   (unsigned)((b * PART_FLOATS + tid * 4) * 4), 0, 16);
            __builtin_amdgcn_sched_barrier(0);
            if (lane == 0) __hip_atomic_fetch_add(&cnt[3], 1u, __ATOMIC_RELAXED, __HIP_MEMORY_SCOPE_WORKGROUP);
            __builtin_amdgcn_sched_barrier(0);
        }
        gu32* pa = (lane < 8) ? (Fw + b * WSTRIDE + lane) : (Xw + (h * NSPLIT + (lane & 7)) * WSTRIDE);
        unsigned val = 0;
        if (w == 0) val = __hip_atomic_load(pa, RLX_AGENT);
        asm volatile("s_waitcnt vmcnt(0)" ::: "memory");
        if (lane == 0) __hip_atomic_fetch_add(&cnt[1], 1u, __ATOMIC_RELEASE, __HIP_MEMORY_SCOPE_WORKGROUP);
        if (w == 0) {
            lds_wait_ge(&cnt[1], 8u);
            STAMP(3);
            gu32* ea = Fw + (h * NSPLIT + (lane & 7)) * WSTRIDE + s;
            const bool echo_lane = (lane >= 8) && (lane < 16) && ((lane & 7) != s);
            if (echo_lane) __hip_atomic_store(ea, val, RLX_AGENT);
            __builtin_amdgcn_sched_barrier(0);
            if (lane == 0) __hip_atomic_fetch_add(&cnt[3], 1u, __ATOMIC_RELAXED, __HIP_MEMORY_SCOPE_WORKGROUP);
            __builtin_amdgcn_sched_barrier(0);
            unsigned last = val;
            for (unsigned spins = 0;; ++spins) {
                bool done = __all((lane >= 8) || (lane == s) || (val == myx));
                if (spins >= SPIN_CAP) done = true;
                if (done) break;
                __builtin_amdgcn_s_sleep(1);
                val = __hip_atomic_load(pa, RLX_AGENT);
                asm volatile("s_waitcnt vmcnt(0)" ::: "memory");
                if (echo_lane && val != last) __hip_atomic_store(ea, val, RLX_AGENT);
                last = val;
            }
            STAMP(4);
            if (lane == 0) __hip_atomic_store(&cnt[2], 1u, __ATOMIC_RELEASE, __HIP_MEMORY_SCOPE_WORKGROUP);
            val = __hip_atomic_load(pa, RLX_AGENT);
            asm volatile("s_waitcnt vmcnt(0)" ::: "memory");
            if (echo_lane && val != last) __hip_atomic_store(ea, val, RLX_AGENT);
        } else {
            lds_wait_ge(&cnt[2], 1u);
        }
        {
            const int tn = tid >> 8, idx4 = tid & 255;
            f32x4 pv[4];
#pragma unroll
            for (int ss = 0; ss < 4; ++ss)
                pv[ss] = __builtin_bit_cast(f32x4, __builtin_amdgcn_raw_buffer_load_b128(
                    prsrc, (unsigned)(((h * NSPLIT + tn * 4 + ss) * PART_FLOATS + idx4 * 4) * 4), 0, 16));
            f32x4 sum = pv[0];
#pragma unroll
            for (int ss = 1; ss < 4; ++ss) sum += pv[ss];
            uint2 pk;
            pk.x = cvt_pk(sum[0], sum[1]);
            pk.y = cvt_pk(sum[2], sum[3]);
            if (tn == 0) {
                const int r = idx4 >> 5, d0 = 4 * (idx4 & 31);
                uint2* ka8 = reinterpret_cast<uint2*>(kaf) + (((d0 >> 5) * 64 + ((d0 >> 3) & 3) * 16 + r) * 2 + ((d0 >> 2) & 1));
                ka8[0] = pk;
                ka8[16] = pk;
            } else {
                const int r = idx4 >> 5, d0 = 4 * (idx4 & 31);
                unsigned short* vb16 = reinterpret_cast<unsigned short*>(vbf) + (((d0 & 7) * 64 + (r >> 2) * 16 + (d0 >> 3)) * 8 + (r & 3));
                vb16[0] = (unsigned short)(pk.x & 0xFFFFu);
                vb16[512] = (unsigned short)(pk.x >> 16);
                vb16[1024] = (unsigned short)(pk.y & 0xFFFFu);
                vb16[1536] = (unsigned short)(pk.y >> 16);
            }
        }
        STAMPW(5, 1);
    } else {
        vbf[(w - 8) * 64 + lane] = (u32x4){0u, 0u, 0u, 0u};
        __syncthreads();
        const int rq = s * ROWS_PER_WG + (w - 8) * 32;
        const u32x4* qp = reinterpret_cast<const u32x4*>(q + ((size_t)(h * SQ + rq + c) * DH + 8 * g));
        u32x4 q1[4];
#pragma unroll
        for (int i = 0; i < 8; ++i) {
            if (i == NA) { __builtin_amdgcn_sched_barrier(0); lds_wait_ge(&cnt[3], 4u); __builtin_amdgcn_sched_barrier(0); }
            if (i == NA + NB) { __builtin_amdgcn_sched_barrier(0); lds_wait_ge(&cnt[3], 5u); __builtin_amdgcn_sched_barrier(0); }
            if (i < 4) q1[i] = __builtin_nontemporal_load(qp + 16 * (DH / 8) + i * 4);
            else qf[i - 4] = __builtin_nontemporal_load(qp + (i - 4) * 4);
        }
#pragma unroll
        for (int u = 0; u < 4; ++u) qs[((w - 8) * 4 + u) * 64 + lane] = q1[u];
        STAMPW(6, 15);
    }
    __syncthreads();
    STAMPW(7, 15);
    const int row0 = s * ROWS_PER_WG + (w & 7) * 32 + (w < 8 ? 16 : 0);
    if (w < 8) {
#pragma unroll
        for (int u = 0; u < 4; ++u) qf[u] = qs[(w * 4 + u) * 64 + lane];
    }

    u32x4 ka[4];
#pragma unroll
    for (int u = 0; u < 4; ++u) ka[u] = kaf[u * 64 + lane];
    u32x4 vb[8];
#pragma unroll
    for (int t = 0; t < 8; ++t) vb[t] = vbf[t * 64 + lane];
    {
        f32x4 sc = {0.f, 0.f, 0.f, 0.f};
#pragma unroll
        for (int u = 0; u < 4; ++u)
            sc = __builtin_amdgcn_mfma_f32_16x16x32_bf16(__builtin_bit_cast(bf16x8, ka[u]), __builtin_bit_cast(bf16x8, qf[u]), sc, 0, 0, 0);
        const float scale = 0.08838834764831845f;
        const unsigned s01 = cvt_pk(sc[0], sc[1]), s23 = cvt_pk(sc[2], sc[3]);
        const unsigned t01 = cvt_pk(lo_f(s01) * scale, hi_f(s01) * scale), t23 = cvt_pk(lo_f(s23) * scale, hi_f(s23) * scale);
        const float sv0 = lo_f(t01), sv1 = hi_f(t01), sv2 = lo_f(t23), sv3 = hi_f(t23);
        float m = fmaxf(fmaxf(sv0, sv1), fmaxf(sv2, sv3));
        {
            auto r2 = __builtin_amdgcn_permlane16_swap(__float_as_uint(m), __float_as_uint(m), false, false);
            m = fmaxf(__uint_as_float(r2[0]), __uint_as_float(r2[1]));
        }
        const float e0 = __expf(sv0 - m), e1 = __expf(sv1 - m), e2 = __expf(sv2 - m), e3 = __expf(sv3 - m);
        float sum = (e0 + e1) + (e2 + e3);
        {
            auto r2 = __builtin_amdgcn_permlane16_swap(__float_as_uint(sum), __float_as_uint(sum), false, false);
            sum = __uint_as_float(r2[0]) + __uint_as_float(r2[1]);
        }
        const float inv = __builtin_amdgcn_rcpf(sum);
        u32x4 pa = {0u, 0u, 0u, 0u};
        pa[0] = (g < 2) ? cvt_pk(e0 * inv, e1 * inv) : 0u;
        pa[1] = (g < 2) ? cvt_pk(e2 * inv, e3 * inv) : 0u;
        f32x4 o[8];
#pragma unroll
        for (int t = 0; t < 8; ++t) {
            f32x4 z = {0.f, 0.f, 0.f, 0.f};
            o[t] = __builtin_amdgcn_mfma_f32_16x16x32_bf16(__builtin_bit_cast(bf16x8, pa), __builtin_bit_cast(bf16x8, vb[t]), z, 0, 0, 0);
        }
        u32x4* op = reinterpret_cast<u32x4*>(out + ((size_t)(h * SQ + row0 + 4 * g) * DH + 8 * c));
#pragma unroll
        for (int i = 0; i < 4; ++i) {
            u32x4 ov;
            ov[0] = cvt_pk(o[0][i], o[1][i]);
            ov[1] = cvt_pk(o[2][i], o[3][i]);
            ov[2] = cvt_pk(o[4][i], o[5][i]);
            ov[3] = cvt_pk(o[6][i], o[7][i]);
            __builtin_nontemporal_store(ov, op + i * (DH / 8));
        }
    }
    STAMPW(8, 15);
    STAMPW(9, 15);
}

extern "C" void kernel_launch(void* const* d_in, const int* in_sizes, int n_in,
                              void* d_out, int out_size, void* d_ws, size_t ws_size,
                              hipStream_t stream) {
    const unsigned short* q = (const unsigned short*)d_in[0];
    const unsigned short* k = (const unsigned short*)d_in[1];
    const unsigned short* v = (const unsigned short*)d_in[2];
    const unsigned short* e = (const unsigned short*)d_in[3];
    const unsigned short* f = (const unsigned short*)d_in[4];
    linformer_fused<<<H * NSPLIT, 1024, 24 * 1024, stream>>>(q, k, v, e, f, (unsigned short*)d_out, (unsigned*)d_ws);
}
```

```cpp
#include <hip/hip_runtime.h>
#include <hip/hip_bf16.h>

typedef __attribute__((ext_vector_type(8))) short bf16x8;
typedef __attribute__((ext_vector_type(4))) float f32x4;
typedef __attribute__((ext_vector_type(4))) unsigned int u32x4;
typedef __attribute__((address_space(1))) unsigned gu32;
#define RLX_AGENT __ATOMIC_RELAXED, __HIP_MEMORY_SCOPE_AGENT

constexpr int H = 32, SQ = 2048, SKV = 2048, DH = 128, R = 8;
constexpr int NSPLIT = 8;
constexpr int ROWS_PER_WG = 256;
constexpr int PART_FLOATS = R * DH;
constexpr size_t PART_BYTES = (size_t)H * NSPLIT * PART_FLOATS * 4;
constexpr size_t XOFF_WORDS = (4u << 20) / 4;
constexpr size_t FOFF_WORDS = (5u << 20) / 4;
constexpr int WSTRIDE = 32;
#define NA 4
#define NB 2
constexpr unsigned SPIN_CAP = 1u << 22;

__device__ __forceinline__ unsigned short f2bf(float f) {
    __hip_bfloat16 b = __float2bfloat16(f);
    return *reinterpret_cast<unsigned short*>(&b);
}
__device__ __forceinline__ float bfround(float f) {
    return __uint_as_float(((unsigned)f2bf(f)) << 16);
}
__device__ __forceinline__ unsigned pack2(float lo, float hi) {
    return (unsigned)f2bf(lo) | ((unsigned)f2bf(hi) << 16);
}
typedef __attribute__((ext_vector_type(2))) float f32x2;
typedef __attribute__((ext_vector_type(2))) __bf16 bf16x2;
__device__ __forceinline__ unsigned cvt_pk(float a, float b) {
    f32x2 v = {a, b};
    return __builtin_bit_cast(unsigned, __builtin_convertvector(v, bf16x2));
}
__device__ __forceinline__ float lo_f(unsigned p) { return __uint_as_float(p << 16); }
__device__ __forceinline__ float hi_f(unsigned p) { return __uint_as_float(p & 0xFFFF0000u); }
__device__ __forceinline__ unsigned perm_lo(unsigned a, unsigned b) {
    return __builtin_amdgcn_perm(b, a, 0x05040100u);
}
__device__ __forceinline__ unsigned perm_hi(unsigned a, unsigned b) {
    return __builtin_amdgcn_perm(b, a, 0x07060302u);
}
__device__ __forceinline__ void lds_wait_ge(unsigned* p, unsigned want) {
#pragma unroll 1
    for (unsigned spins = 0; spins < SPIN_CAP; ++spins) {
        if (__builtin_amdgcn_readfirstlane(__hip_atomic_load(p, __ATOMIC_ACQUIRE, __HIP_MEMORY_SCOPE_WORKGROUP)) >= want) return;
        __builtin_amdgcn_s_sleep(1);
    }
}

#define STAMP(i)
#define STAMPW(i, ww)

__global__ __launch_bounds__(1024) void linformer_fused(const unsigned short* __restrict__ q,
                                                        const unsigned short* __restrict__ k,
                                                        const unsigned short* __restrict__ v,
                                                        const unsigned short* __restrict__ e,
                                                        const unsigned short* __restrict__ f,
                                                        unsigned short* __restrict__ out,
                                                        unsigned* __restrict__ ws) {
    __shared__ __attribute__((aligned(16))) float red[8 * R * DH];
    __shared__ u32x4 kaf[4 * 64];
    __shared__ uint2 vbf[8 * 32];
    __shared__ unsigned cnt[4];
    __shared__ u32x4 qs[8 * 4 * 64];
    const int tid = threadIdx.x, lane = tid & 63;
    const int w = __builtin_amdgcn_readfirstlane(tid >> 6);
    const int c = lane & 15, g = lane >> 4;
    const int b = blockIdx.x, h = b >> 3, s = b & 7;
    STAMP(0);
    if (tid < 4) cnt[tid] = 0u;
    u32x4 qf[4];

    if (w < 8) {
        gu32* Xw = (gu32*)ws + XOFF_WORDS;
        gu32* Fw = (gu32*)ws + FOFF_WORDS;
        const __amdgpu_buffer_rsrc_t prsrc = __builtin_amdgcn_make_buffer_rsrc(ws, 0, (int)PART_BYTES, 0x00020000);
        const __amdgpu_buffer_rsrc_t wrsrc = __builtin_amdgcn_make_buffer_rsrc(ws, 0, 8 << 20, 0x00020000);
        gu32* a0 = (lane < 8) ? (Fw + b * WSTRIDE + lane) : (Xw + b * WSTRIDE);
        const unsigned oldv = __hip_atomic_load(a0, RLX_AGENT);

        const int tensor = s >> 2;
        const unsigned short* src = tensor ? v : k;
        const unsigned short* proj = tensor ? f : e;
        const int nbase = (s & 3) * 512 + w * 64;
        const __amdgpu_buffer_rsrc_t ersrc = __builtin_amdgcn_make_buffer_rsrc((void*)proj, 0, R * SKV * 2, 0x00020000);
        u32x4 a[2];
#pragma unroll
        for (int st = 0; st < 2; ++st)
            a[st] = __builtin_amdgcn_raw_buffer_load_b128(ersrc, (unsigned)((c * SKV + nbase + 32 * st + 8 * g) * 2), 0, 0);
        const u32x4* p = reinterpret_cast<const u32x4*>(src + ((size_t)(h * SKV + nbase + 8 * g) * DH + 8 * c));
        u32x4 x[16];
#pragma unroll
        for (int j = 0; j < 16; ++j) x[j] = __builtin_nontemporal_load(p + ((j >> 3) * 32 + (j & 7)) * (DH / 8));

        unsigned myx = __builtin_amdgcn_readlane(oldv, 8) + 1u;
        while (__any(oldv == myx)) ++myx;
        __builtin_amdgcn_raw_buffer_store_b32(myx, wrsrc, tid == 0 ? (unsigned)((XOFF_WORDS + b * WSTRIDE) * 4) : 0xFFFFFFF0u, 0, 16);
        __builtin_amdgcn_sched_barrier(0);
        __syncthreads();
        __builtin_amdgcn_sched_barrier(0);

        f32x4 acc[8];
#pragma unroll
        for (int t = 0; t < 8; ++t) acc[t] = (f32x4){0.f, 0.f, 0.f, 0.f};
#pragma unroll
        for (int st = 0; st < 2; ++st) {
#pragma unroll
            for (int t = 0; t < 8; ++t) {
                u32x4 bb;
#pragma unroll
                for (int qq = 0; qq < 4; ++qq) {
                    unsigned lo = x[8 * st + 2 * qq][t >> 1], hi = x[8 * st + 2 * qq + 1][t >> 1];
                    bb[qq] = (t & 1) ? perm_hi(lo, hi) : perm_lo(lo, hi);
                }
                acc[t] = __builtin_amdgcn_mfma_f32_16x16x32_bf16(__builtin_bit_cast(bf16x8, a[st]), __builtin_bit_cast(bf16x8, bb), acc[t], 0, 0, 0);
            }
        }
        if (g < 2) {
#pragma unroll
            for (int i = 0; i < 4; ++i) {
                float* dst = red + (w * R + 4 * g + i) * DH + 8 * c;
                f32x4 v0 = {acc[0][i], acc[1][i], acc[2][i], acc[3][i]};
                f32x4 v1 = {acc[4][i], acc[5][i], acc[6][i], acc[7][i]};
                *reinterpret_cast<f32x4*>(dst) = v0;
                *reinterpret_cast<f32x4*>(dst + 4) = v1;
            }
        }
        STAMP(1);
        if (lane == 0) __hip_atomic_fetch_add(&cnt[0], 1u, __ATOMIC_RELEASE, __HIP_MEMORY_SCOPE_WORKGROUP);
        lds_wait_ge(&cnt[0], 8u);
        STAMP(2);
        if (w < 4) {
            f32x4 sum = *reinterpret_cast<const f32x4*>(red + tid * 4);
#pragma unroll
            for (int ww = 1; ww < 8; ++ww)
                sum += *reinterpret_cast<const f32x4*>(red + (ww * R * DH) + tid * 4);
            __builtin_amdgcn_raw_buffer_store_b128(__builtin_bit_cast(u32x4, sum), prsrc,
                                                   (unsigned)((b * PART_FLOATS + tid * 4) * 4), 0, 16);
            __builtin_amdgcn_sched_barrier(0);
            if (lane == 0) __hip_atomic_fetch_add(&cnt[3], 1u, __ATOMIC_RELAXED, __HIP_MEMORY_SCOPE_WORKGROUP);
            __builtin_amdgcn_sched_barrier(0);
        }
        gu32* pa = (lane < 8) ? (Fw + b * WSTRIDE + lane) : (Xw + (h * NSPLIT + (lane & 7)) * WSTRIDE);
        unsigned val = 0;
        if (w == 0) val = __hip_atomic_load(pa, RLX_AGENT);
        asm volatile("s_waitcnt vmcnt(0)" ::: "memory");
        if (lane == 0) __hip_atomic_fetch_add(&cnt[1], 1u, __ATOMIC_RELEASE, __HIP_MEMORY_SCOPE_WORKGROUP);
        if (w == 0) {
            lds_wait_ge(&cnt[1], 8u);
            STAMP(3);
            gu32* ea = Fw + (h * NSPLIT + (lane & 7)) * WSTRIDE + s;
            const bool echo_lane = (lane >= 8) && (lane < 16) && ((lane & 7) != s);
            if (echo_lane) __hip_atomic_store(ea, val, RLX_AGENT);
            __builtin_amdgcn_sched_barrier(0);
            if (lane == 0) __hip_atomic_fetch_add(&cnt[3], 1u, __ATOMIC_RELAXED, __HIP_MEMORY_SCOPE_WORKGROUP);
            __builtin_amdgcn_sched_barrier(0);
            unsigned last = val;
            for (unsigned spins = 0;; ++spins) {
                bool done = __all((lane >= 8) || (lane == s) || (val == myx));
                if (spins >= SPIN_CAP) done = true;
                if (done) break;
                __builtin_amdgcn_s_sleep(1);
                val = __hip_atomic_load(pa, RLX_AGENT);
                asm volatile("s_waitcnt vmcnt(0)" ::: "memory");
                if (echo_lane && val != last) __hip_atomic_store(ea, val, RLX_AGENT);
                last = val;
            }
            STAMP(4);
            if (lane == 0) __hip_atomic_store(&cnt[2], 1u, __ATOMIC_RELEASE, __HIP_MEMORY_SCOPE_WORKGROUP);
            val = __hip_atomic_load(pa, RLX_AGENT);
            asm volatile("s_waitcnt vmcnt(0)" ::: "memory");
            if (echo_lane && val != last) __hip_atomic_store(ea, val, RLX_AGENT);
        } else {
            lds_wait_ge(&cnt[2], 1u);
        }
        {
            const int tn = tid >> 8, idx4 = tid & 255;
            f32x4 pv[4];
#pragma unroll
            for (int ss = 0; ss < 4; ++ss)
                pv[ss] = __builtin_bit_cast(f32x4, __builtin_amdgcn_raw_buffer_load_b128(
                    prsrc, (unsigned)(((h * NSPLIT + tn * 4 + ss) * PART_FLOATS + idx4 * 4) * 4), 0, 16));
            f32x4 sum = pv[0];
#pragma unroll
            for (int ss = 1; ss < 4; ++ss) sum += pv[ss];
            uint2 pk;
            pk.x = cvt_pk(sum[0], sum[1]);
            pk.y = cvt_pk(sum[2], sum[3]);
            if (tn == 0) {
                const int r = idx4 >> 5, d0 = 4 * (idx4 & 31);
                uint2* ka8 = reinterpret_cast<uint2*>(kaf) + (((d0 >> 5) * 64 + ((d0 >> 3) & 3) * 16 + r) * 2 + ((d0 >> 2) & 1));
                ka8[0] = pk;
                ka8[16] = pk;
            } else {
                const int r = idx4 >> 5, d0 = 4 * (idx4 & 31);
                unsigned short* vb16 = reinterpret_cast<unsigned short*>(vbf) + (((d0 & 7) * 32 + (r >> 2) * 16 + (d0 >> 3)) * 4 + (r & 3));
                vb16[0] = (unsigned short)(pk.x & 0xFFFFu);
                vb16[128] = (unsigned short)(pk.x >> 16);
                vb16[256] = (unsigned short)(pk.y & 0xFFFFu);
                vb16[384] = (unsigned short)(pk.y >> 16);
            }
        }
        STAMPW(5, 1);
    } else {
        __syncthreads();
        const int rq = s * ROWS_PER_WG + (w - 8) * 32;
        const u32x4* qp = reinterpret_cast<const u32x4*>(q + ((size_t)(h * SQ + rq + c) * DH + 8 * g));
        u32x4 q1[4];
#pragma unroll
        for (int i = 0; i < 8; ++i) {
            if (i == NA) { __builtin_amdgcn_sched_barrier(0); lds_wait_ge(&cnt[3], 4u); __builtin_amdgcn_sched_barrier(0); }
            if (i == NA + NB) { __builtin_amdgcn_sched_barrier(0); lds_wait_ge(&cnt[3], 5u); __builtin_amdgcn_sched_barrier(0); }
            if (i < 4) q1[i] = __builtin_nontemporal_load(qp + 16 * (DH / 8) + i * 4);
            else qf[i - 4] = __builtin_nontemporal_load(qp + (i - 4) * 4);
        }
#pragma unroll
        for (int u = 0; u < 4; ++u) qs[((w - 8) * 4 + u) * 64 + lane] = q1[u];
        STAMPW(6, 15);
    }
    __syncthreads();
    STAMPW(7, 15);
    const int row0 = s * ROWS_PER_WG + (w & 7) * 32 + (w < 8 ? 16 : 0);
    if (w < 8) {
#pragma unroll
        for (int u = 0; u < 4; ++u) qf[u] = qs[(w * 4 + u) * 64 + lane];
    }

    u32x4 ka[4];
#pragma unroll
    for (int u = 0; u < 4; ++u) ka[u] = kaf[u * 64 + lane];
    u32x4 vb[8];
#pragma unroll
    for (int t = 0; t < 8; ++t) {
        const uint2 lo = vbf[t * 32 + (lane & 31)];
        vb[t] = (u32x4){lo.x, lo.y, 0u, 0u};
    }
    {
        f32x4 sc = {0.f, 0.f, 0.f, 0.f};
#pragma unroll
        for (int u = 0; u < 4; ++u)
            sc = __builtin_amdgcn_mfma_f32_16x16x32_bf16(__builtin_bit_cast(bf16x8, ka[u]), __builtin_bit_cast(bf16x8, qf[u]), sc, 0, 0, 0);
        const float scale = 0.08838834764831845f;
        const unsigned s01 = cvt_pk(sc[0], sc[1]), s23 = cvt_pk(sc[2], sc[3]);
        const unsigned t01 = cvt_pk(lo_f(s01) * scale, hi_f(s01) * scale), t23 = cvt_pk(lo_f(s23) * scale, hi_f(s23) * scale);
        const float sv0 = lo_f(t01), sv1 = hi_f(t01), sv2 = lo_f(t23), sv3 = hi_f(t23);
        float m = fmaxf(fmaxf(sv0, sv1), fmaxf(sv2, sv3));
        {
            auto r2 = __builtin_amdgcn_permlane16_swap(__float_as_uint(m), __float_as_uint(m), false, false);
            m = fmaxf(__uint_as_float(r2[0]), __uint_as_float(r2[1]));
        }
        const float e0 = __expf(sv0 - m), e1 = __expf(sv1 - m), e2 = __expf(sv2 - m), e3 = __expf(sv3 - m);
        float sum = (e0 + e1) + (e2 + e3);
        {
            auto r2 = __builtin_amdgcn_permlane16_swap(__float_as_uint(sum), __float_as_uint(sum), false, false);
            sum = __uint_as_float(r2[0]) + __uint_as_float(r2[1]);
        }
        const float inv = __builtin_amdgcn_rcpf(sum);
        u32x4 pa = {0u, 0u, 0u, 0u};
        pa[0] = (g < 2) ? cvt_pk(e0 * inv, e1 * inv) : 0u;
        pa[1] = (g < 2) ? cvt_pk(e2 * inv, e3 * inv) : 0u;
        f32x4 o[8];
#pragma unroll
        for (int t = 0; t < 8; ++t) {
            f32x4 z = {0.f, 0.f, 0.f, 0.f};
            o[t] = __builtin_amdgcn_mfma_f32_16x16x32_bf16(__builtin_bit_cast(bf16x8, pa), __builtin_bit_cast(bf16x8, vb[t]), z, 0, 0, 0);
        }
        u32x4* op = reinterpret_cast<u32x4*>(out + ((size_t)(h * SQ + row0 + 4 * g) * DH + 8 * c));
#pragma unroll
        for (int i = 0; i < 4; ++i) {
            u32x4 ov;
            ov[0] = cvt_pk(o[0][i], o[1][i]);
            ov[1] = cvt_pk(o[2][i], o[3][i]);
            ov[2] = cvt_pk(o[4][i], o[5][i]);
            ov[3] = cvt_pk(o[6][i], o[7][i]);
            __builtin_nontemporal_store(ov, op + i * (DH / 8));
        }
    }
    STAMPW(8, 15);
    STAMPW(9, 15);
}

extern "C" void kernel_launch(void* const* d_in, const int* in_sizes, int n_in,
                              void* d_out, int out_size, void* d_ws, size_t ws_size,
                              hipStream_t stream) {
    const unsigned short* q = (const unsigned short*)d_in[0];
    const unsigned short* k = (const unsigned short*)d_in[1];
    const unsigned short* v = (const unsigned short*)d_in[2];
    const unsigned short* e = (const unsigned short*)d_in[3];
    const unsigned short* f = (const unsigned short*)d_in[4];
    linformer_fused<<<H * NSPLIT, 1024, 24 * 1024, stream>>>(q, k, v, e, f, (unsigned short*)d_out, (unsigned*)d_ws);
}
```

```cpp
#include <hip/hip_runtime.h>
#include <hip/hip_bf16.h>

typedef __attribute__((ext_vector_type(8))) short bf16x8;
typedef __attribute__((ext_vector_type(4))) float f32x4;
typedef __attribute__((ext_vector_type(2))) float f32x2;
typedef __attribute__((ext_vector_type(2))) __bf16 bf16x2;
typedef __attribute__((ext_vector_type(4))) unsigned int u32x4;
typedef __attribute__((address_space(1))) unsigned gu32;
#define RLX_AGENT __ATOMIC_RELAXED, __HIP_MEMORY_SCOPE_AGENT

constexpr int H = 32, SQ = 2048, SKV = 2048, DH = 128, R = 8;
constexpr int NG = 8;
constexpr int QROWS = 256;
constexpr int PART_FLOATS = R * DH;
constexpr size_t PART_BYTES = (size_t)H * NG * PART_FLOATS * 4;
constexpr size_t XOFF_WORDS = (4u << 20) / 4;
constexpr size_t FOFF_WORDS = (5u << 20) / 4;
constexpr int WSTRIDE = 32;
constexpr unsigned SPIN_CAP = 1u << 21;

__device__ __forceinline__ unsigned cvt_pk(float a, float b) {
    f32x2 v = {a, b};
    return __builtin_bit_cast(unsigned, __builtin_convertvector(v, bf16x2));
}
__device__ __forceinline__ float lo_f(unsigned p) { return __uint_as_float(p << 16); }
__device__ __forceinline__ float hi_f(unsigned p) { return __uint_as_float(p & 0xFFFF0000u); }
__device__ __forceinline__ unsigned perm_lo(unsigned a, unsigned b) {
    return __builtin_amdgcn_perm(b, a, 0x05040100u);
}
__device__ __forceinline__ unsigned perm_hi(unsigned a, unsigned b) {
    return __builtin_amdgcn_perm(b, a, 0x07060302u);
}
__device__ __forceinline__ void lds_wait_ge(unsigned* p, unsigned want) {
#pragma unroll 1
    for (unsigned spins = 0; spins < SPIN_CAP; ++spins) {
        if (__builtin_amdgcn_readfirstlane(__hip_atomic_load(p, __ATOMIC_ACQUIRE, __HIP_MEMORY_SCOPE_WORKGROUP)) >= want) return;
        __builtin_amdgcn_s_sleep(1);
    }
}

__global__ __launch_bounds__(1024) void linformer_fused(const unsigned short* __restrict__ q,
                                                        const unsigned short* __restrict__ k,
                                                        const unsigned short* __restrict__ v,
                                                        const unsigned short* __restrict__ e,
                                                        const unsigned short* __restrict__ f,
                                                        unsigned short* __restrict__ out,
                                                        unsigned* __restrict__ ws) {
    __shared__ __attribute__((aligned(16))) float red[8 * R * DH];
    __shared__ u32x4 kaf[4 * 64];
    __shared__ uint2 vbf[8 * 32];
    __shared__ unsigned cnt[4];
    __shared__ u32x4 qs[8 * 4 * 64];
    const int tid = threadIdx.x, lane = tid & 63;
    const int w = __builtin_amdgcn_readfirstlane(tid >> 6);
    const int c = lane & 15, g = lane >> 4;
    const int b = blockIdx.x, h = b >> 3, s = b & 7;
    if (tid < 4) cnt[tid] = 0u;
    u32x4 qf[4];

    if (w < 8) {
        gu32* Xw = (gu32*)ws + XOFF_WORDS;
        gu32* Fw = (gu32*)ws + FOFF_WORDS;
        const __amdgpu_buffer_rsrc_t prsrc = __builtin_amdgcn_make_buffer_rsrc(ws, 0, (int)PART_BYTES, 0x00020000);
        const __amdgpu_buffer_rsrc_t wrsrc = __builtin_amdgcn_make_buffer_rsrc(ws, 0, 8 << 20, 0x00020000);
        gu32* a0 = (lane < 8) ? (Fw + b * WSTRIDE + lane) : (Xw + b * WSTRIDE);
        const unsigned oldv = __hip_atomic_load(a0, RLX_AGENT);

        const int tensor = s >> 2;
        const unsigned short* src = tensor ? v : k;
        const unsigned short* proj = tensor ? f : e;
        const int nbase = (s & 3) * 512 + w * 64;
        const __amdgpu_buffer_rsrc_t ersrc = __builtin_amdgcn_make_buffer_rsrc((void*)proj, 0, R * SKV * 2, 0x00020000);
        u32x4 a[2];
#pragma unroll
        for (int st = 0; st < 2; ++st)
            a[st] = __builtin_amdgcn_raw_buffer_load_b128(ersrc, (unsigned)((c * SKV + nbase + 32 * st + 8 * g) * 2), 0, 0);
        const u32x4* p = reinterpret_cast<const u32x4*>(src + ((size_t)(h * SKV + nbase + 8 * g) * DH + 8 * c));
        u32x4 x[16];
#pragma unroll
        for (int j = 0; j < 16; ++j) x[j] = __builtin_nontemporal_load(p + ((j >> 3) * 32 + (j & 7)) * (DH / 8));

        unsigned myx = __builtin_amdgcn_readlane(oldv, 8) + 1u;
        while (__any(oldv == myx)) ++myx;
        __builtin_amdgcn_raw_buffer_store_b32(myx, wrsrc, tid == 0 ? (unsigned)((XOFF_WORDS + b * WSTRIDE) * 4) : 0xFFFFFFF0u, 0, 16);
        __builtin_amdgcn_sched_barrier(0);
        __syncthreads();
        __builtin_amdgcn_sched_barrier(0);

        f32x4 acc[8];
#pragma unroll
        for (int t = 0; t < 8; ++t) acc[t] = (f32x4){0.f, 0.f, 0.f, 0.f};
#pragma unroll
        for (int st = 0; st < 2; ++st) {
#pragma unroll
            for (int t = 0; t < 8; ++t) {
                u32x4 bb;
#pragma unroll
                for (int qq = 0; qq < 4; ++qq) {
                    unsigned lo = x[8 * st + 2 * qq][t >> 1], hi = x[8 * st + 2 * qq + 1][t >> 1];
                    bb[qq] = (t & 1) ? perm_hi(lo, hi) : perm_lo(lo, hi);
                }
                acc[t] = __builtin_amdgcn_mfma_f32_16x16x32_bf16(__builtin_bit_cast(bf16x8, a[st]), __builtin_bit_cast(bf16x8, bb), acc[t], 0, 0, 0);
            }
        }
        if (g < 2) {
#pragma unroll
            for (int i = 0; i < 4; ++i) {
                float* dst = red + (w * R + 4 * g + i) * DH + 8 * c;
                f32x4 v0 = {acc[0][i], acc[1][i], acc[2][i], acc[3][i]};
                f32x4 v1 = {acc[4][i], acc[5][i], acc[6][i], acc[7][i]};
                *reinterpret_cast<f32x4*>(dst) = v0;
                *reinterpret_cast<f32x4*>(dst + 4) = v1;
            }
        }
        if (lane == 0) __hip_atomic_fetch_add(&cnt[0], 1u, __ATOMIC_RELEASE, __HIP_MEMORY_SCOPE_WORKGROUP);
        lds_wait_ge(&cnt[0], 8u);
        if (w < 4) {
            f32x4 sum = *reinterpret_cast<const f32x4*>(red + tid * 4);
#pragma unroll
            for (int ww = 1; ww < 8; ++ww)
                sum += *reinterpret_cast<const f32x4*>(red + (ww * R * DH) + tid * 4);
            __builtin_amdgcn_raw_buffer_store_b128(__builtin_bit_cast(u32x4, sum), prsrc,
                                                   (unsigned)((b * PART_FLOATS + tid * 4) * 4), 0, 16);
            __builtin_amdgcn_sched_barrier(0);
            if (lane == 0) __hip_atomic_fetch_add(&cnt[3], 1u, __ATOMIC_RELAXED, __HIP_MEMORY_SCOPE_WORKGROUP);
            __builtin_amdgcn_sched_barrier(0);
        }
        gu32* pa = (lane < 8) ? (Fw + b * WSTRIDE + lane) : (Xw + (h * NG + (lane & 7)) * WSTRIDE);
        unsigned val = 0;
        if (w == 0) val = __hip_atomic_load(pa, RLX_AGENT);
        asm volatile("s_waitcnt vmcnt(0)" ::: "memory");
        if (lane == 0) __hip_atomic_fetch_add(&cnt[1], 1u, __ATOMIC_RELEASE, __HIP_MEMORY_SCOPE_WORKGROUP);
        gu32* ea = Fw + (h * NG + (lane & 7)) * WSTRIDE + s;
        const bool echo_lane = (w == 0) && (lane >= 8) && (lane < 16) && ((lane & 7) != s);
        unsigned last = val;
        if (w == 0) {
            lds_wait_ge(&cnt[1], 8u);
            if (echo_lane) __hip_atomic_store(ea, val, RLX_AGENT);
#pragma unroll 1
            for (unsigned spins = 0; spins < SPIN_CAP; ++spins) {
                if (__all((lane >= 8) || (lane == s) || (val == myx))) break;
                __builtin_amdgcn_s_sleep(1);
                val = __hip_atomic_load(pa, RLX_AGENT);
                asm volatile("s_waitcnt vmcnt(0)" ::: "memory");
                if (echo_lane && val != last) __hip_atomic_store(ea, val, RLX_AGENT);
                last = val;
            }
            if (lane == 0) __hip_atomic_store(&cnt[2], 1u, __ATOMIC_RELEASE, __HIP_MEMORY_SCOPE_WORKGROUP);
        } else {
            lds_wait_ge(&cnt[2], 1u);
        }
        val = __hip_atomic_load(pa, RLX_AGENT);
        {
            const int tn = tid >> 8, idx4 = tid & 255;
            f32x4 pv[4];
#pragma unroll
            for (int ss = 0; ss < 4; ++ss)
                pv[ss] = __builtin_bit_cast(f32x4, __builtin_amdgcn_raw_buffer_load_b128(
                    prsrc, (unsigned)(((h * NG + tn * 4 + ss) * PART_FLOATS + idx4 * 4) * 4), 0, 16));
            f32x4 sum = pv[0];
#pragma unroll
            for (int ss = 1; ss < 4; ++ss) sum += pv[ss];
            uint2 pk;
            pk.x = cvt_pk(sum[0], sum[1]);
            pk.y = cvt_pk(sum[2], sum[3]);
            const int r = idx4 >> 5, d0 = 4 * (idx4 & 31);
            if (tn == 0) {
                uint2* ka8 = reinterpret_cast<uint2*>(kaf) + (((d0 >> 5) * 64 + ((d0 >> 3) & 3) * 16 + r) * 2 + ((d0 >> 2) & 1));
                ka8[0] = pk;
                ka8[16] = pk;
            } else {
                unsigned short* vb16 = reinterpret_cast<unsigned short*>(vbf) + (((d0 & 7) * 32 + (r >> 2) * 16 + (d0 >> 3)) * 4 + (r & 3));
                vb16[0] = (unsigned short)(pk.x & 0xFFFFu);
                vb16[128] = (unsigned short)(pk.x >> 16);
                vb16[256] = (unsigned short)(pk.y & 0xFFFFu);
                vb16[384] = (unsigned short)(pk.y >> 16);
            }
        }
        if (echo_lane && val != last) __hip_atomic_store(ea, val, RLX_AGENT);
    } else {
        __syncthreads();
        const int rq = s * QROWS + (w - 8) * 32;
        const u32x4* qp = reinterpret_cast<const u32x4*>(q + ((size_t)(h * SQ + rq + c) * DH + 8 * g));
        u32x4 q1[4];
#pragma unroll
        for (int u = 0; u < 4; ++u) q1[u] = __builtin_nontemporal_load(qp + 16 * (DH / 8) + u * 4);
        __builtin_amdgcn_sched_barrier(0);
        lds_wait_ge(&cnt[3], 4u);
        __builtin_amdgcn_sched_barrier(0);
#pragma unroll
        for (int u = 0; u < 4; ++u) qf[u] = __builtin_nontemporal_load(qp + u * 4);
#pragma unroll
        for (int u = 0; u < 4; ++u) qs[((w - 8) * 4 + u) * 64 + lane] = q1[u];
    }
    __syncthreads();

    const int row0 = s * QROWS + (w & 7) * 32 + (w < 8 ? 16 : 0);
    if (w < 8) {
#pragma unroll
        for (int u = 0; u < 4; ++u) qf[u] = qs[(w * 4 + u) * 64 + lane];
    }
    u32x4 ka[4];
#pragma unroll
    for (int u = 0; u < 4; ++u) ka[u] = kaf[u * 64 + lane];
    u32x4 vb[8];
#pragma unroll
    for (int t = 0; t < 8; ++t) {
        const uint2 lo = vbf[t * 32 + (lane & 31)];
        vb[t] = (u32x4){lo.x, lo.y, 0u, 0u};
    }
    f32x4 sc = {0.f, 0.f, 0.f, 0.f};
#pragma unroll
    for (int u = 0; u < 4; ++u)
        sc = __builtin_amdgcn_mfma_f32_16x16x32_bf16(__builtin_bit_cast(bf16x8, ka[u]), __builtin_bit_cast(bf16x8, qf[u]), sc, 0, 0, 0);
    const float scale = 0.08838834764831845f;
    const unsigned s01 = cvt_pk(sc[0], sc[1]), s23 = cvt_pk(sc[2], sc[3]);
    const unsigned t01 = cvt_pk(lo_f(s01) * scale, hi_f(s01) * scale), t23 = cvt_pk(lo_f(s23) * scale, hi_f(s23) * scale);
    const float sv0 = lo_f(t01), sv1 = hi_f(t01), sv2 = lo_f(t23), sv3 = hi_f(t23);
    float m = fmaxf(fmaxf(sv0, sv1), fmaxf(sv2, sv3));
    {
        auto r2 = __builtin_amdgcn_permlane16_swap(__float_as_uint(m), __float_as_uint(m), false, false);
        m = fmaxf(__uint_as_float(r2[0]), __uint_as_float(r2[1]));
    }
    const float e0 = __expf(sv0 - m), e1 = __expf(sv1 - m), e2 = __expf(sv2 - m), e3 = __expf(sv3 - m);
    float sum = (e0 + e1) + (e2 + e3);
    {
        auto r2 = __builtin_amdgcn_permlane16_swap(__float_as_uint(sum), __float_as_uint(sum), false, false);
        sum = __uint_as_float(r2[0]) + __uint_as_float(r2[1]);
    }
    const float inv = __builtin_amdgcn_rcpf(sum);
    u32x4 pa = {0u, 0u, 0u, 0u};
    pa[0] = (g < 2) ? cvt_pk(e0 * inv, e1 * inv) : 0u;
    pa[1] = (g < 2) ? cvt_pk(e2 * inv, e3 * inv) : 0u;
    f32x4 o[8];
#pragma unroll
    for (int t = 0; t < 8; ++t) {
        f32x4 z = {0.f, 0.f, 0.f, 0.f};
        o[t] = __builtin_amdgcn_mfma_f32_16x16x32_bf16(__builtin_bit_cast(bf16x8, pa), __builtin_bit_cast(bf16x8, vb[t]), z, 0, 0, 0);
    }
    u32x4* op = reinterpret_cast<u32x4*>(out + ((size_t)(h * SQ + row0 + 4 * g) * DH + 8 * c));
#pragma unroll
    for (int i = 0; i < 4; ++i) {
        u32x4 ov;
        ov[0] = cvt_pk(o[0][i], o[1][i]);
        ov[1] = cvt_pk(o[2][i], o[3][i]);
        ov[2] = cvt_pk(o[4][i], o[5][i]);
        ov[3] = cvt_pk(o[6][i], o[7][i]);
        __builtin_nontemporal_store(ov, op + i * (DH / 8));
    }
}

extern "C" void kernel_launch(void* const* d_in, const int* in_sizes, int n_in,
                              void* d_out, int out_size, void* d_ws, size_t ws_size,
                              hipStream_t stream) {
    const unsigned short* q = (const unsigned short*)d_in[0];
    const unsigned short* k = (const unsigned short*)d_in[1];
    const unsigned short* v = (const unsigned short*)d_in[2];
    const unsigned short* e = (const unsigned short*)d_in[3];
    const unsigned short* f = (const unsigned short*)d_in[4];
    linformer_fused<<<H * NG, 1024, 24 * 1024, stream>>>(q, k, v, e, f, (unsigned short*)d_out, (unsigned*)d_ws);
}
```
